```python
import torch

def _softmax(x, axis=-1):
    return torch.softmax(x, dim=axis)

def _gelu(x):
    return torch.nn.functional.gelu(x, approximate='tanh')

def _silu(x):
    return torch.nn.functional.silu(x)

def _sigmoid(x):
    return torch.sigmoid(x)

def _layernorm(x, axis=-1, eps=1e-05):
    m = x.mean(dim=axis, keepdim=True)
    v = x.var(dim=axis, keepdim=True, unbiased=False)
    return (x - m) / torch.sqrt(v + eps)

def _scatter_add(tgt, idx, src, axis=0):
    return tgt.clone().index_add_(axis, idx, src)

def _segment_sum(src, seg, n):
    out = torch.zeros((n,) + src.shape[1:], dtype=src.dtype)
    return out.index_add_(0, seg, src)

def _pad(x, pw):
    flat = tuple((v for pair in reversed(pw) for v in pair))
    return torch.nn.functional.pad(x, flat)
kernel_name = 'adaLN_cond__ssm_conv_h3__glu_gated_swiglu__prompt'

def setup_inputs():

    _w = lambda a: (lambda t: t.to(torch.bfloat16) if t.is_floating_point() else t)(torch.as_tensor(a))

    def _A_setup():
        B, S, D = (1, 2048, 1024)
        ks = 4
        return {'q': _w((lambda *s: torch.randn(*s, dtype=torch.bfloat16) * 0.05)(B, S, D)), 'k': _w((lambda *s: torch.randn(*s, dtype=torch.bfloat16) * 0.8)(B, S, D)), 'v': _w((lambda *s: torch.randn(*s, dtype=torch.bfloat16) * 0.8)(B, S, D)), 'shift_k': _w((lambda *s: torch.randn(*s, dtype=torch.bfloat16) * 0.8)(D, ks) * 0.5), 'ssm_k': _w((lambda *s: torch.randn(*s, dtype=torch.bfloat16) * 0.8)(D, S) * (1.0 / S ** 0.5)), 'ssm_D': _w((lambda *s: torch.randn(*s, dtype=torch.bfloat16) * 0.8)(D))}
    _A_in = _A_setup()

    def _B_setup():
        B, S, D, F = (1, 2048, 1024, 3584)
        return {'x': _w((lambda *s: torch.randn(*s, dtype=torch.bfloat16) * 0.05)(B, S, D)), 'w1': _w((lambda *s: torch.randn(*s, dtype=torch.bfloat16) * 0.8)(D, F) * (2.0 / D) ** 0.5), 'w3': _w((lambda *s: torch.randn(*s, dtype=torch.bfloat16) * 0.8)(D, F) * (2.0 / D) ** 0.5), 'w2': _w((lambda *s: torch.randn(*s, dtype=torch.bfloat16) * 0.8)(F, D) * (2.0 / F) ** 0.5)}
    _B_in = _B_setup()
    return {'A_q': _A_in['q'], 'A_k': _A_in['k'], 'A_v': _A_in['v'], 'A_shift_k': _A_in['shift_k'], 'A_ssm_k': _A_in['ssm_k'], 'A_ssm_D': _A_in['ssm_D'], 'B_x': _B_in['x'], 'B_w1': _B_in['w1'], 'B_w3': _B_in['w3'], 'B_w2': _B_in['w2'], 'c': _w((lambda *s: torch.randn(*s, dtype=torch.bfloat16) * 0.2)(1, 1024) * 8.0), 'w_mod': _w((lambda *s: torch.randn(*s, dtype=torch.bfloat16) * 0.2)(1024, 6 * 1024) * 1024 ** (-0.5) * 8.0)}

def reference(A_q, A_k, A_v, A_shift_k, A_ssm_k, A_ssm_D, B_x, B_w1, B_w3, B_w2, c, w_mod):

    def _A_ref(q, k, v, shift_k, ssm_k, ssm_D):
        B, S, D = q.shape
        ks = shift_k.shape[-1]
        k_pad = _pad(k, ((0, 0), (ks - 1, 0), (0, 0)))
        k_shift = torch.zeros_like(k)
        for t in range(ks):
            k_shift = k_shift + k_pad[:, t:t + S, :] * shift_k[:, ks - 1 - t][None, None, :]
        kv = k_shift * v
        L = 2 * S
        kv_pad = _pad(kv, ((0, 0), (0, S), (0, 0)))
        kf_pad = _pad(ssm_k, ((0, 0), (0, S)))
        kv_f = (lambda x, axes=(-2, -1): torch.fft.rfft2(x.float(), dim=axes))(kv_pad[:, None, :, :], axes=(1, 2))
        kf_f = (lambda x, axes=(-2, -1): torch.fft.rfft2(x.float(), dim=axes))(kf_pad[:, None, :], axes=(1, 2))
        kf_f = (lambda x, *d: x.permute(*d))(kf_f, 1, 2, 0)
        y_f = kv_f * kf_f[None, :, :, :]
        y = (lambda x, s, axes=(-2, -1): torch.fft.irfft2(x, s=s, dim=axes).to(torch.bfloat16))(y_f, (1, L), axes=(1, 2))[:, 0, :S, :]
        y = y + kv * ssm_D[None, None, :]
        return q * y

    def _B_ref(x, w1, w3, w2):
        gate = torch.einsum('bsd,df->bsf', x, w1)
        up = torch.einsum('bsd,df->bsf', x, w3)
        h = _silu(gate) * up
        out = torch.einsum('bsf,fd->bsd', h, w2)
        return out
    x = A_q
    mod = (_silu(c) @ w_mod).reshape(c.shape[0], 6, 1, -1)
    sa, ca, ga, sb, cb, gb = (mod[:, 0].clone(), mod[:, 1].clone(), mod[:, 2].clone(), mod[:, 3].clone(), mod[:, 4].clone(), mod[:, 5].clone())
    h = _layernorm(x, axis=-1) * (1 + ca) + sa
    x = x + ga * _A_ref(h, A_k, A_v, A_shift_k, A_ssm_k, A_ssm_D)
    h = _layernorm(x, axis=-1) * (1 + cb) + sb
    x = x + gb * _B_ref(h, B_w1, B_w3, B_w2)
    return x
```

```cpp
#include <hip/hip_runtime.h>
#include <hip/hip_bf16.h>
#include <stdint.h>


typedef unsigned short u16;
typedef unsigned int u32;
typedef __attribute__((ext_vector_type(8))) __bf16 bf16x8;
typedef __attribute__((ext_vector_type(2))) __bf16 bf16x2;
typedef __attribute__((ext_vector_type(4))) short s16x4;
typedef __attribute__((ext_vector_type(8))) short s16x8;
typedef __attribute__((ext_vector_type(4))) float f32x4;
typedef __attribute__((ext_vector_type(2))) float f32x2;
typedef __attribute__((ext_vector_type(4))) u32 u32x4;
typedef __attribute__((ext_vector_type(2))) u32 u32x2;

#define S_ 2048
#define D_ 1024
#define F_ 3584

#define WS_KVT (0u)
#define WS_XT (4u << 20)
#define WS_X1T (8u << 20)
#define WS_APT (12u << 20)
#define WS_HHT (16u << 20)
#define WS_MODP (32u << 20)
#define WS_ST1 (33u << 20)
#define WS_MOD ((33u << 20) + (64u << 10))
#define WS_ST2 ((33u << 20) + (128u << 10))
#define WS_C12 ((33u << 20) + (192u << 10))

__device__ __forceinline__ float bflo(u32 u) { return __uint_as_float(u << 16); }
__device__ __forceinline__ float bfhi(u32 u) { return __uint_as_float(u & 0xffff0000u); }
__device__ __forceinline__ float bf1(u16 h) { return __uint_as_float(((u32)h) << 16); }
__device__ __forceinline__ u32 pk2(float a, float b) {
  bf16x2 r = {(__bf16)a, (__bf16)b};
  return __builtin_bit_cast(u32, r);
}
__device__ __forceinline__ float rbf(float a) {
  return __uint_as_float(((u32)__builtin_bit_cast(u16, (__bf16)a)) << 16);
}
__device__ __forceinline__ float silu_f(float g) { return g * __builtin_amdgcn_rcpf(1.0f + __expf(-g)); }

#define GLDS16(gptr, ldsptr)                                                        \
  __builtin_amdgcn_global_load_lds((const __attribute__((address_space(1))) u32*)(gptr), \
                                   (__attribute__((address_space(3))) u32*)(ldsptr), 16, 0, 0)
#define WAIT_V(n) asm volatile("s_waitcnt vmcnt(" #n ")" ::: "memory")
#define WAIT_L(n) asm volatile("s_waitcnt lgkmcnt(" #n ")" ::: "memory")
#define BAR __builtin_amdgcn_s_barrier()
#define SCHED __builtin_amdgcn_sched_barrier(0)

__device__ __forceinline__ float wave_sum(float v) {
#pragma unroll
  for (int o = 32; o > 0; o >>= 1) v += __shfl_xor(v, o, 64);
  return v;
}

#define K0_MODB 384
#define K0_STB 256
#define K0_TRB 256
__global__ __launch_bounds__(256) void k0_prep(const u16* __restrict__ x, const u16* __restrict__ kk,
                                               const u16* __restrict__ vv, const u16* __restrict__ shk,
                                               const u16* __restrict__ cvec, const u16* __restrict__ wmod,
                                               u16* __restrict__ kvT, u16* __restrict__ xT,
                                               f32x2* __restrict__ st1, float* __restrict__ modp) {
  __shared__ float red[8 * 256];
  const int tid = threadIdx.x;
  int b = blockIdx.x;
  if (b < K0_TRB) {
    const int dt = b & 15, stile = b >> 4;
    const int lane = tid & 63, w = tid >> 6;
    const int c8 = lane & 7, sq = lane >> 3;
    const int s0 = stile * 128 + w * 32 + sq * 4;
    const int d0 = dt * 64 + c8 * 8;
    u32x4 kr[7], vr[4], xr[4], sw[4];
#pragma unroll
    for (int i = 0; i < 7; ++i) {
      int s = s0 - 3 + i;
      int sc = s < 0 ? 0 : s;
      kr[i] = *(const u32x4*)(kk + (size_t)sc * D_ + d0);
    }
#pragma unroll
    for (int i = 0; i < 4; ++i) vr[i] = *(const u32x4*)(vv + (size_t)(s0 + i) * D_ + d0);
#pragma unroll
    for (int i = 0; i < 4; ++i) xr[i] = *(const u32x4*)(x + (size_t)(s0 + i) * D_ + d0);
#pragma unroll
    for (int i = 0; i < 4; ++i) sw[i] = *(const u32x4*)(shk + (size_t)d0 * 4 + i * 8);
    if (s0 == 0) {
#pragma unroll
      for (int i = 0; i < 3; ++i) kr[i] = (u32x4){0, 0, 0, 0};
    }
#pragma unroll
    for (int j = 0; j < 8; ++j) {
      u32 wa = sw[j >> 1][(j & 1) * 2], wb = sw[j >> 1][(j & 1) * 2 + 1];
      float w0 = bflo(wa), w1 = bfhi(wa), w2 = bflo(wb), w3 = bfhi(wb);
      float kf[7];
#pragma unroll
      for (int i = 0; i < 7; ++i) kf[i] = (j & 1) ? bfhi(kr[i][j >> 1]) : bflo(kr[i][j >> 1]);
      float o[4];
#pragma unroll
      for (int i = 0; i < 4; ++i) {
        float ks = kf[i] * w3;
        ks = fmaf(kf[i + 1], w2, ks);
        ks = fmaf(kf[i + 2], w1, ks);
        ks = fmaf(kf[i + 3], w0, ks);
        float vf = (j & 1) ? bfhi(vr[i][j >> 1]) : bflo(vr[i][j >> 1]);
        o[i] = ks * vf;
      }
      u32x2 ov = {pk2(o[0], o[1]), pk2(o[2], o[3])};
      *(u32x2*)(kvT + (size_t)(d0 + j) * S_ + s0) = ov;
      const u32 sel = (j & 1) ? 0x07060302u : 0x05040100u;
      u32x2 xv = {__builtin_amdgcn_perm(xr[1][j >> 1], xr[0][j >> 1], sel),
                  __builtin_amdgcn_perm(xr[3][j >> 1], xr[2][j >> 1], sel)};
      *(u32x2*)(xT + (size_t)(d0 + j) * S_ + s0) = xv;
    }
    return;
  }
  b -= K0_TRB;
  if (b < K0_MODB) {
    const int ks = b / 24, strip = b % 24;
    const int cl = tid & 31, rg = tid >> 5;
    u32x4 wv[8];
    u16 cv[8];
#pragma unroll
    for (int i = 0; i < 8; ++i) {
      int row = ks * 64 + rg + 8 * i;
      wv[i] = *(const u32x4*)(wmod + (size_t)row * (6 * D_) + strip * 256 + cl * 8);
      cv[i] = cvec[row];
    }
    float acc[8];
#pragma unroll
    for (int j = 0; j < 8; ++j) acc[j] = 0.f;
#pragma unroll
    for (int i = 0; i < 8; ++i) {
      float cf = bf1(cv[i]);
      float sc = rbf(cf / (1.0f + __expf(-cf)));
#pragma unroll
      for (int j = 0; j < 4; ++j) {
        acc[2 * j] = fmaf(sc, bflo(wv[i][j]), acc[2 * j]);
        acc[2 * j + 1] = fmaf(sc, bfhi(wv[i][j]), acc[2 * j + 1]);
      }
    }
    *(f32x4*)&red[rg * 256 + cl * 8] = (f32x4){acc[0], acc[1], acc[2], acc[3]};
    *(f32x4*)&red[rg * 256 + cl * 8 + 4] = (f32x4){acc[4], acc[5], acc[6], acc[7]};
    __syncthreads();
    float s = 0.f;
#pragma unroll
    for (int i = 0; i < 8; ++i) s += red[i * 256 + tid];
    modp[ks * (6 * D_) + strip * 256 + tid] = s;
    return;
  }
  b -= K0_MODB;
  {
    const int lane = tid & 63, w = tid >> 6;
    const int r0 = b * 8 + w * 2;
    u32x4 a[2][2];
#pragma unroll
    for (int r = 0; r < 2; ++r)
#pragma unroll
      for (int h = 0; h < 2; ++h) a[r][h] = *(const u32x4*)(x + (size_t)(r0 + r) * D_ + h * 512 + lane * 8);
#pragma unroll
    for (int r = 0; r < 2; ++r) {
      float s = 0.f;
#pragma unroll
      for (int h = 0; h < 2; ++h)
#pragma unroll
        for (int j = 0; j < 4; ++j) s += bflo(a[r][h][j]) + bfhi(a[r][h][j]);
      s = wave_sum(s);
      float m = s * (1.0f / D_);
      float q = 0.f;
#pragma unroll
      for (int h = 0; h < 2; ++h)
#pragma unroll
        for (int j = 0; j < 4; ++j) {
          float d0 = bflo(a[r][h][j]) - m, d1 = bfhi(a[r][h][j]) - m;
          q = fmaf(d0, d0, q);
          q = fmaf(d1, d1, q);
        }
      q = wave_sum(q);
      float rs = rsqrtf(q * (1.0f / D_) + 1e-5f);
      if (lane == 0) st1[r0 + r] = (f32x2){m, rs};
    }
  }
}

#define K1_KOFF 0
#define K1_UOFF 4224
#define K1_XOFF 8576
#define K1_YOFF 12672
#define K1_WSZ 16768
#define K1_STOFF (4 * K1_WSZ)
#define K1_LDS (K1_STOFF + 16384)

typedef __attribute__((address_space(3))) s16x4* lds_s16x4p;
__device__ __forceinline__ bf16x8 tr_frag(const char* p0, const char* p1) {
  s16x4 a = __builtin_amdgcn_ds_read_tr16_b64_v4i16((lds_s16x4p)p0);
  s16x4 b = __builtin_amdgcn_ds_read_tr16_b64_v4i16((lds_s16x4p)p1);
  s16x8 c = __builtin_shufflevector(a, b, 0, 1, 2, 3, 4, 5, 6, 7);
  return __builtin_bit_cast(bf16x8, c);
}
__device__ __forceinline__ float ror1(float v) {
  return __builtin_bit_cast(float, __builtin_amdgcn_update_dpp(0, __builtin_bit_cast(int, v), 0x13C, 0xf, 0xf, false));
}

__global__ __launch_bounds__(256, 1) void k1_conv(const u16* __restrict__ ssmk, const u16* __restrict__ ssmD,
                                                  const u16* __restrict__ kvT, const u16* __restrict__ xT,
                                                  const f32x2* __restrict__ st1, const float* __restrict__ modp,
                                                  u16* __restrict__ x1T, u16* __restrict__ apT,
                                                  float* __restrict__ modf) {
  extern __shared__ __attribute__((aligned(16))) char smem[];
  const int tid = threadIdx.x, lane = tid & 63;
  const int w = __builtin_amdgcn_readfirstlane(tid >> 6);
  const int ch = blockIdx.x * 4 + w;
  char* wb = smem + w * K1_WSZ;
#pragma unroll
  for (int i = 0; i < 4; ++i) {
    GLDS16(ssmk + (size_t)ch * S_ + i * 512 + lane * 8, wb + K1_KOFF + 128 + i * 1024);
    GLDS16(kvT + (size_t)ch * S_ + i * 512 + lane * 8, wb + K1_UOFF + 256 + i * 1024);
  }
#pragma unroll
  for (int i = 0; i < 4; ++i) {
    GLDS16(xT + (size_t)ch * S_ + i * 512 + lane * 8, wb + K1_XOFF + i * 1024);
    GLDS16((const char*)st1 + (w * 4 + i) * 1024 + lane * 16, smem + K1_STOFF + (w * 4 + i) * 1024);
  }
  if (lane < 8) *(u32x4*)(wb + K1_KOFF + lane * 16) = (u32x4){0, 0, 0, 0};
  else if (lane < 24) *(u32x4*)(wb + K1_UOFF + (lane - 8) * 16) = (u32x4){0, 0, 0, 0};
  float mv0 = modp[(lane & 15) * (6 * D_) + (lane >> 4) * D_ + ch];
  float mv1 = modp[(lane & 15) * (6 * D_) + (4 + ((lane >> 4) & 1)) * D_ + ch];
  const float dd = bf1(ssmD[ch]);
#pragma unroll
  for (int o = 8; o > 0; o >>= 1) {
    mv0 += __shfl_xor(mv0, o, 64);
    mv1 += __shfl_xor(mv1, o, 64);
  }
  mv0 = rbf(mv0);
  mv1 = rbf(mv1);
  const float sa = __shfl(mv0, 0, 64), ca1 = rbf(1.0f + __shfl(mv0, 16, 64)), ga = __shfl(mv0, 32, 64);
  const float sb = __shfl(mv0, 48, 64), cb1 = rbf(1.0f + __shfl(mv1, 0, 64)), gb = __shfl(mv1, 16, 64);
  if (lane == 0) {
    modf[3 * D_ + ch] = sb;
    modf[4 * D_ + ch] = cb1;
    modf[5 * D_ + ch] = gb;
  }
  WAIT_V(0);
  __syncthreads();

  const int G = lane >> 4, q = (lane >> 2) & 3, p = lane & 3;
  const char* abase = wb + K1_KOFF + 128 - 96 + 32 * p + 8 * (8 * G + q);
  const char* bbase = wb + K1_UOFF + 256 + 96 + 8 * p - 8 * (8 * G + q);
  const bool l63 = (lane == 63);

  bf16x8 Af[16], Bf[32];
  float p3 = 0.f, p2 = 0.f, p1 = 0.f;
  u16* yb = (u16*)(wb + K1_YOFF);
#pragma unroll
  for (int a = 0; a < 16; ++a) {
    Af[a] = tr_frag(abase + 256 * a, abase + 256 * a + 32);
    Bf[2 * a] = tr_frag(bbase + 128 * (2 * a), bbase + 128 * (2 * a) - 32);
    Bf[2 * a + 1] = tr_frag(bbase + 128 * (2 * a + 1), bbase + 128 * (2 * a + 1) - 32);
    f32x4 z0 = {0, 0, 0, 0}, z1 = {0, 0, 0, 0};
#pragma unroll
    for (int i = 0; i <= a; ++i) {
      z0 = __builtin_amdgcn_mfma_f32_16x16x32_bf16(Af[i], Bf[2 * a - 2 * i], z0, 0, 0, 0);
      z1 = __builtin_amdgcn_mfma_f32_16x16x32_bf16(Af[i], Bf[2 * a + 1 - 2 * i], z1, 0, 0, 0);
    }
#pragma unroll
    for (int h = 0; h < 2; ++h) {
      f32x4 z = h ? z1 : z0;
      float w3 = z[3];
      float w2 = ror1(l63 ? p3 : w3) + z[2];
      float w1 = ror1(l63 ? p2 : w2) + z[1];
      float y = ror1(l63 ? p1 : w1) + z[0];
      p3 = w3; p2 = w2; p1 = w1;
      yb[64 * (2 * a + h) + lane] = __builtin_bit_cast(u16, (__bf16)y);
    }
    if ((a & 3) == 3) {
      const int c = a >> 2;
      const int so = c * 512 + lane * 8;
      u32x4 yv = *(const u32x4*)(wb + K1_YOFF + so * 2);
      u32x4 uv = *(const u32x4*)(wb + K1_UOFF + 256 + so * 2);
      u32x4 xv = *(const u32x4*)(wb + K1_XOFF + so * 2);
      u32x4 o1, o2;
#pragma unroll
      for (int j = 0; j < 4; ++j) {
        f32x4 st = *(const f32x4*)(smem + K1_STOFF + (so + 2 * j) * 8);
        float r[2], ap[2];
#pragma unroll
        for (int e = 0; e < 2; ++e) {
          float xf = e ? bfhi(xv[j]) : bflo(xv[j]);
          float yf = e ? bfhi(yv[j]) : bflo(yv[j]);
          float uf = e ? bfhi(uv[j]) : bflo(uv[j]);
          float m1 = st[2 * e], r1 = st[2 * e + 1];
          float h1 = fmaf((xf - m1) * r1, ca1, sa);
          float yy = fmaf(uf, dd, yf);
          r[e] = rbf(fmaf(ga, h1 * yy, xf));
          ap[e] = (r[e] - m1) * cb1;
        }
        o1[j] = pk2(r[0], r[1]);
        o2[j] = pk2(ap[0], ap[1]);
      }
      *(u32x4*)(x1T + (size_t)ch * S_ + so) = o1;
      *(u32x4*)(apT + (size_t)ch * S_ + so) = o2;
    }
  }
}

__global__ __launch_bounds__(256) void k2_aux(const u16* __restrict__ x1T, const f32x2* __restrict__ st1,
                                              const float* __restrict__ modf, const u16* __restrict__ w1,
                                              const u16* __restrict__ w3, f32x2* __restrict__ st2,
                                              float* __restrict__ c12) {
  __shared__ float red[9 * 224 * 2];
  const int tid = threadIdx.x, lane = tid & 63, w = tid >> 6;
  int b = blockIdx.x;
  if (b < 256) {
    const int s0 = b * 8;
    u32x4 a[4];
#pragma unroll
    for (int i = 0; i < 4; ++i) a[i] = *(const u32x4*)(x1T + (size_t)(tid + 256 * i) * S_ + s0);
    float sm[8];
#pragma unroll
    for (int j = 0; j < 4; ++j) {
      sm[2 * j] = 0.f; sm[2 * j + 1] = 0.f;
#pragma unroll
      for (int i = 0; i < 4; ++i) { sm[2 * j] += bflo(a[i][j]); sm[2 * j + 1] += bfhi(a[i][j]); }
    }
#pragma unroll
    for (int j = 0; j < 8; ++j) sm[j] = wave_sum(sm[j]);
    if (lane == 0) {
#pragma unroll
      for (int j = 0; j < 8; ++j) red[w * 8 + j] = sm[j];
    }
    __syncthreads();
    float m[8];
#pragma unroll
    for (int j = 0; j < 8; ++j) m[j] = (red[j] + red[8 + j] + red[16 + j] + red[24 + j]) * (1.0f / D_);
#pragma unroll
    for (int j = 0; j < 4; ++j) {
      sm[2 * j] = 0.f; sm[2 * j + 1] = 0.f;
#pragma unroll
      for (int i = 0; i < 4; ++i) {
        float d0 = bflo(a[i][j]) - m[2 * j], d1 = bfhi(a[i][j]) - m[2 * j + 1];
        sm[2 * j] = fmaf(d0, d0, sm[2 * j]); sm[2 * j + 1] = fmaf(d1, d1, sm[2 * j + 1]);
      }
    }
#pragma unroll
    for (int j = 0; j < 8; ++j) sm[j] = wave_sum(sm[j]);
    if (lane == 0) {
#pragma unroll
      for (int j = 0; j < 8; ++j) red[32 + w * 8 + j] = sm[j];
    }
    __syncthreads();
    if (tid < 8) {
      float q = (red[32 + tid] + red[40 + tid] + red[48 + tid] + red[56 + tid]) * (1.0f / D_);
      float mm = (red[tid] + red[8 + tid] + red[16 + tid] + red[24 + tid]) * (1.0f / D_);
      float r2 = rsqrtf(q + 1e-5f);
      st2[s0 + tid] = (f32x2){r2, r2 * (mm - st1[s0 + tid][0])};
    }
    return;
  }
  b -= 256;
  {
    const int ks = b >> 5, nt = b & 31;
    const int cidx = tid % 28, rg = tid / 28;
    float a1[8], a2[8];
#pragma unroll
    for (int j = 0; j < 8; ++j) { a1[j] = 0.f; a2[j] = 0.f; }
    if (rg < 9) {
      const u16* wp = (cidx < 14 ? w1 : w3) + nt * 112 + (cidx % 14) * 8;
      for (int r = rg; r < 128; r += 9) {
        int row = ks * 128 + r;
        u32x4 wv = *(const u32x4*)(wp + (size_t)row * F_);
        float s1 = modf[4 * D_ + row], s2 = modf[3 * D_ + row];
#pragma unroll
        for (int j = 0; j < 4; ++j) {
          a1[2 * j] = fmaf(s1, bflo(wv[j]), a1[2 * j]); a1[2 * j + 1] = fmaf(s1, bfhi(wv[j]), a1[2 * j + 1]);
          a2[2 * j] = fmaf(s2, bflo(wv[j]), a2[2 * j]); a2[2 * j + 1] = fmaf(s2, bfhi(wv[j]), a2[2 * j + 1]);
        }
      }
#pragma unroll
      for (int j = 0; j < 8; ++j) {
        red[(rg * 2 + 0) * 224 + cidx * 8 + j] = a1[j];
        red[(rg * 2 + 1) * 224 + cidx * 8 + j] = a2[j];
      }
    }
    __syncthreads();
    for (int i = tid; i < 448; i += 256) {
      float s = 0.f;
#pragma unroll
      for (int g = 0; g < 9; ++g) s += red[g * 448 + i];
      c12[(ks * 32 + nt) * 448 + i] = s;
    }
  }
}

#define G1_SLOT 32768
#define G1_R 5
#define G1_BOFF 16384
#define G1_LDS (G1_R * G1_SLOT)

#define TRRD(dst, addr, off) \
  asm volatile("ds_read_b64_tr_b16 %0, %1 offset:%2" : "=v"(dst) : "v"(addr), "i"(off))

__global__ __launch_bounds__(512, 2) void g1_gemm(const u16* __restrict__ apT, const u16* __restrict__ w1,
                                                  const u16* __restrict__ w3, const f32x2* __restrict__ st2,
                                                  const float* __restrict__ c12, u16* __restrict__ hhT) {
  extern __shared__ __attribute__((aligned(16))) char smem[];
  const int tid = threadIdx.x, lane = tid & 63;
  const int w = __builtin_amdgcn_readfirstlane(tid >> 6);
  const int grp = w >> 2;
  const int wm = w & 3, wn = grp;
  const int b = blockIdx.x;
  const int xcd = b & 7, idx = b >> 3;
  const int mt = idx & 7, nt = xcd * 4 + (idx >> 3);
  const int s0 = mt * 256, f0 = nt * 112;

  const char* asrc[2];
  const char* bsrc[2];
#pragma unroll
  for (int j = 0; j < 2; ++j) {
    int row = 4 * w + 2 * j + (lane >> 5);
    int c = (lane & 31) ^ ((row & 7) << 1);
    asrc[j] = (const char*)apT + (size_t)row * (S_ * 2) + s0 * 2 + c * 16;
    int xx = j * 64 + lane;
    if (xx > 111) xx = 111;
    int brow = 4 * w + xx / 28;
    int cc = (xx % 28) ^ (w & 1);
    bsrc[j] = (const char*)(cc < 14 ? w1 : w3) + (size_t)brow * (F_ * 2) + f0 * 2 + (cc % 14) * 16;
  }
  const int adst = w * 2048, bdst = G1_BOFF + w * 2048;
#define G1_STAGE(slot)                                                   \
  do {                                                                   \
    char* sb_ = smem + (slot) * G1_SLOT;                                 \
    GLDS16(asrc[0], sb_ + adst);                                         \
    GLDS16(asrc[1], sb_ + adst + 1024);                                  \
    GLDS16(bsrc[0], sb_ + bdst);                                         \
    GLDS16(bsrc[1], sb_ + bdst + 1024);                                  \
    asrc[0] += 32 * S_ * 2; asrc[1] += 32 * S_ * 2;                      \
    bsrc[0] += 32 * F_ * 2; bsrc[1] += 32 * F_ * 2;                      \
  } while (0)

  const int g = lane >> 4, q = (lane >> 2) & 3, p = lane & 3;
  u32 ard[4], brd[2];
  {
    const int row = 4 * g + q;
#pragma unroll
    for (int m = 0; m < 4; ++m) {
      int c = (8 * wm + 2 * m + (p >> 1)) ^ ((row & 7) << 1);
      ard[m] = row * 512 + c * 16 + 8 * (p & 1);
    }
#pragma unroll
    for (int n = 0; n < 2; ++n) {
      int c = (14 * (p >> 1) + 7 * wn + n) ^ (g & 1);
      brd[n] = G1_BOFF + g * 2048 + q * 448 + c * 16 + 8 * (p & 1);
    }
  }
  const u32 lds0 = (u32)(uintptr_t)(__attribute__((address_space(3))) char*)smem;
#pragma unroll
  for (int m = 0; m < 4; ++m) ard[m] += lds0;
  brd[0] += lds0; brd[1] += lds0;

  f32x4 acc[4][7];
#pragma unroll
  for (int m = 0; m < 4; ++m)
#pragma unroll
    for (int n = 0; n < 7; ++n) acc[m][n] = (f32x4){0, 0, 0, 0};

#pragma unroll
  for (int i = 0; i < G1_R - 1; ++i) G1_STAGE(i);
  WAIT_V(12);
  BAR;
  if (grp == 1) BAR;

  int rslot = 0;
  int wslot = G1_R - 1;
  for (int t = 0; t < 32; ++t) {
    u32x2 af[4][2], bf[7][2];
    const u32 so = rslot;
#pragma unroll
    for (int n = 0; n < 7; ++n) {
      u32 ad = brd[n & 1] + so;
      TRRD(bf[n][0], ad, (n >> 1) * 32);
      TRRD(bf[n][1], ad, (n >> 1) * 32 + 8192);
    }
#pragma unroll
    for (int m = 0; m < 4; ++m) {
      u32 ad = ard[m] + so;
      TRRD(af[m][0], ad, 0);
      TRRD(af[m][1], ad, 8192);
    }
    if (t + G1_R - 1 < 32) {
      G1_STAGE(wslot);
      WAIT_L(0);
      if (grp == 1) WAIT_V(12);
    } else {
      WAIT_L(0);
      if (grp == 1) WAIT_V(0);
    }
    rslot += G1_SLOT; if (rslot == G1_R * G1_SLOT) rslot = 0;
    wslot += 1; if (wslot == G1_R) wslot = 0;
    SCHED;
    BAR;
    SCHED;
    __builtin_amdgcn_s_setprio(1);
#pragma unroll
    for (int m = 0; m < 4; ++m) {
      u32x4 a4 = {af[m][0][0], af[m][0][1], af[m][1][0], af[m][1][1]};
#pragma unroll
      for (int n = 0; n < 7; ++n) {
        u32x4 b4 = {bf[n][0][0], bf[n][0][1], bf[n][1][0], bf[n][1][1]};
        acc[m][n] = __builtin_amdgcn_mfma_f32_16x16x32_bf16(__builtin_bit_cast(bf16x8, a4),
                                                            __builtin_bit_cast(bf16x8, b4), acc[m][n], 0, 0, 0);
      }
    }
    __builtin_amdgcn_s_setprio(0);
    if (grp == 0) {
      if (t + G1_R - 1 < 32) WAIT_V(12); else WAIT_V(0);
    }
    SCHED;
    BAR;
    SCHED;
  }
  if (grp == 0) BAR;

  {
    const int j = lane & 15;
    const bool lo = j < 8;
    float c1[7], c2[7];
    {
      const int cbase = (lo ? 0 : 112) + wn * 56 + (j & 7);
#pragma unroll
      for (int n = 0; n < 7; ++n) {
        float s1 = 0.f, s2 = 0.f;
#pragma unroll
        for (int ks = 0; ks < 8; ++ks) {
          s1 += c12[(ks * 32 + nt) * 448 + cbase + 8 * n];
          s2 += c12[(ks * 32 + nt) * 448 + 224 + cbase + 8 * n];
        }
        c1[n] = s1; c2[n] = s2;
      }
    }
    f32x2 st[4][4];
#pragma unroll
    for (int m = 0; m < 4; ++m)
#pragma unroll
      for (int r = 0; r < 4; ++r) st[m][r] = st2[s0 + wm * 64 + 16 * m + 4 * g + r];
#pragma unroll
    for (int n = 0; n < 7; ++n) {
      float v[4][4];
#pragma unroll
      for (int m = 0; m < 4; ++m)
#pragma unroll
        for (int r = 0; r < 4; ++r) v[m][r] = fmaf(acc[m][n][r], st[m][r][0], fmaf(-st[m][r][1], c1[n], c2[n]));
      u32 o[2][2];
#pragma unroll
      for (int mm = 0; mm < 2; ++mm) {
        float h[4];
#pragma unroll
        for (int r = 0; r < 4; ++r) {
          float snd = lo ? v[mm + 2][r] : v[mm][r];
          float rcv = __builtin_bit_cast(float, __builtin_amdgcn_update_dpp(0, __builtin_bit_cast(int, snd), 0x128, 0xf, 0xf, false));
          float gt = lo ? v[mm][r] : rcv;
          float up = lo ? rcv : v[mm + 2][r];
          h[r] = silu_f(gt) * up;
        }
        o[mm][0] = pk2(h[0], h[1]);
        o[mm][1] = pk2(h[2], h[3]);
      }
      const int f = f0 + wn * 56 + 8 * n + (j & 7);
      u16* dst = hhT + (size_t)f * S_ + s0 + wm * 64 + (lo ? 0 : 32) + 4 * g;
      *(u32x2*)(dst) = (u32x2){o[0][0], o[0][1]};
      *(u32x2*)(dst + 16) = (u32x2){o[1][0], o[1][1]};
    }
  }
}

#define G2_SLOT 12288
#define G2_WSZ (3 * G2_SLOT)
#define G2_LDS (4 * G2_WSZ)

__global__ __launch_bounds__(256, 1) void g2_gemm(const u16* __restrict__ hhT, const u16* __restrict__ w2,
                                                  const u16* __restrict__ x1T, const float* __restrict__ modf,
                                                  u16* __restrict__ out) {
  extern __shared__ __attribute__((aligned(16))) char smem[];
  const int tid = threadIdx.x, lane = tid & 63;
  const int w = __builtin_amdgcn_readfirstlane(tid >> 6);
  const int b = blockIdx.x;
  const int xcd = b & 7, idx = b >> 3;
  const int mt = (xcd >> 1) * 4 + (idx & 3), nt = (xcd & 1) * 8 + (idx >> 2);
  const int s0 = mt * 128, d0 = nt * 64;
  char* wb = smem + w * G2_WSZ;

  u32 aoff[2], boff;
  {
#pragma unroll
    for (int h = 0; h < 2; ++h) {
      int r = 4 * h + (lane >> 4);
      aoff[h] = (u32)(r * (S_ * 2) + (((lane & 15) ^ (r << 1)) * 16));
    }
    int r = lane >> 3;
    boff = (u32)(r * (D_ * 2) + (((lane & 7) ^ (((r >> 1) & 3) << 1)) * 16));
  }
  const char* abase = (const char*)hhT + (size_t)(32 * w) * (S_ * 2) + s0 * 2;
  const char* bbase = (const char*)w2 + (size_t)(32 * w) * (D_ * 2) + d0 * 2;
#define G2_STAGE(slot)                                                              \
  do {                                                                              \
    char* sb_ = wb + (slot) * G2_SLOT;                                              \
    _Pragma("unroll") for (int i_ = 0; i_ < 8; ++i_)                                \
        GLDS16(abase + (size_t)(i_ >> 1) * (8 * S_ * 2) + aoff[i_ & 1], sb_ + i_ * 1024); \
    _Pragma("unroll") for (int i_ = 0; i_ < 4; ++i_)                                \
        GLDS16(bbase + (size_t)i_ * (8 * D_ * 2) + boff, sb_ + 8192 + i_ * 1024);   \
    abase += (size_t)128 * S_ * 2;                                                  \
    bbase += (size_t)128 * D_ * 2;                                                  \
  } while (0)

  const int g = lane >> 4, q = (lane >> 2) & 3, p = lane & 3;
  const u32 lds0 = (u32)(uintptr_t)(__attribute__((address_space(3))) char*)wb;
  u32 ard[8], brd[4];
  {
    const int row = 4 * g + q;
#pragma unroll
    for (int m = 0; m < 8; ++m) {
      int c = (2 * m + (p >> 1)) ^ ((row & 7) << 1);
      ard[m] = lds0 + row * 256 + c * 16 + 8 * (p & 1);
    }
#pragma unroll
    for (int n = 0; n < 4; ++n) {
      int c = (2 * n + (p >> 1)) ^ (((row >> 1) & 3) << 1);
      brd[n] = lds0 + 8192 + row * 128 + c * 16 + 8 * (p & 1);
    }
  }
  f32x4 acc[8][4];
#pragma unroll
  for (int m = 0; m < 8; ++m)
#pragma unroll
    for (int n = 0; n < 4; ++n) acc[m][n] = (f32x4){0, 0, 0, 0};

  G2_STAGE(0); G2_STAGE(1); G2_STAGE(2);
#define G2_STEP(slot, DOSTAGE, WV)                                                   \
  do {                                                                               \
    u32x2 af[8][2], bf[4][2];                                                        \
    WAIT_V(WV);                                                                      \
    _Pragma("unroll") for (int n = 0; n < 4; ++n) {                                  \
      TRRD(bf[n][0], brd[n], (slot) * G2_SLOT);                                      \
      TRRD(bf[n][1], brd[n], (slot) * G2_SLOT + 16 * 128);                           \
    }                                                                                \
    _Pragma("unroll") for (int m = 0; m < 8; ++m) {                                  \
      TRRD(af[m][0], ard[m], (slot) * G2_SLOT);                                      \
      TRRD(af[m][1], ard[m], (slot) * G2_SLOT + 16 * 256);                           \
    }                                                                                \
    WAIT_L(0);                                                                       \
    SCHED;                                                                           \
    if (DOSTAGE) G2_STAGE(slot);                                                     \
    _Pragma("unroll") for (int m = 0; m < 8; ++m) {                                  \
      u32x4 a4 = {af[m][0][0], af[m][0][1], af[m][1][0], af[m][1][1]};               \
      _Pragma("unroll") for (int n = 0; n < 4; ++n) {                                \
        u32x4 b4 = {bf[n][0][0], bf[n][0][1], bf[n][1][0], bf[n][1][1]};             \
        acc[m][n] = __builtin_amdgcn_mfma_f32_16x16x32_bf16(                         \
            __builtin_bit_cast(bf16x8, a4), __builtin_bit_cast(bf16x8, b4), acc[m][n], 0, 0, 0); \
      }                                                                              \
    }                                                                                \
    SCHED;                                                                           \
  } while (0)
  for (int t = 0; t < 8; ++t) {
    G2_STEP(0, true, 24);
    G2_STEP(1, true, 24);
    G2_STEP(2, true, 24);
  }
  G2_STEP(0, true, 24);
  G2_STEP(1, false, 24);
  G2_STEP(2, false, 12);
  G2_STEP(0, false, 0);

  __syncthreads();
  f32x4* rb = (f32x4*)smem;
#pragma unroll
  for (int m = 0; m < 8; ++m)
#pragma unroll
    for (int n = 0; n < 4; ++n) rb[((w * 8 + m) * 4 + n) * 64 + lane] = acc[m][n];
  __syncthreads();
  f32x4 fin[2][4];
#pragma unroll
  for (int mm = 0; mm < 2; ++mm)
#pragma unroll
    for (int n = 0; n < 4; ++n) {
      f32x4 s = rb[((0 * 8 + 2 * w + mm) * 4 + n) * 64 + lane];
#pragma unroll
      for (int sw = 1; sw < 4; ++sw) s += rb[((sw * 8 + 2 * w + mm) * 4 + n) * 64 + lane];
      fin[mm][n] = s;
    }
#pragma unroll
  for (int n = 0; n < 4; ++n) {
    const int d = d0 + 16 * n + (lane & 15);
    const float gb = modf[5 * D_ + d];
#pragma unroll
    for (int mm = 0; mm < 2; ++mm) {
      const int s = s0 + 32 * w + 16 * mm + 4 * g;
      u32x2 xv = *(const u32x2*)(x1T + (size_t)d * S_ + s);
      float o0 = fmaf(gb, fin[mm][n][0], bflo(xv[0])), o1 = fmaf(gb, fin[mm][n][1], bfhi(xv[0]));
      float o2 = fmaf(gb, fin[mm][n][2], bflo(xv[1])), o3 = fmaf(gb, fin[mm][n][3], bfhi(xv[1]));
      out[(size_t)(s + 0) * D_ + d] = __builtin_bit_cast(u16, (__bf16)o0);
      out[(size_t)(s + 1) * D_ + d] = __builtin_bit_cast(u16, (__bf16)o1);
      out[(size_t)(s + 2) * D_ + d] = __builtin_bit_cast(u16, (__bf16)o2);
      out[(size_t)(s + 3) * D_ + d] = __builtin_bit_cast(u16, (__bf16)o3);
    }
  }
}

extern "C" void kernel_launch(void* const* d_in, const int* in_sizes, int n_in, void* d_out, int out_size,
                              void* d_ws, size_t ws_size, hipStream_t stream) {
  const u16* x = (const u16*)d_in[0];
  const u16* k = (const u16*)d_in[1];
  const u16* v = (const u16*)d_in[2];
  const u16* shk = (const u16*)d_in[3];
  const u16* ssmk = (const u16*)d_in[4];
  const u16* ssmD = (const u16*)d_in[5];
  const u16* w1 = (const u16*)d_in[7];
  const u16* w3 = (const u16*)d_in[8];
  const u16* w2 = (const u16*)d_in[9];
  const u16* c = (const u16*)d_in[10];
  const u16* wmod = (const u16*)d_in[11];
  char* ws = (char*)d_ws;
  u16* kvT = (u16*)(ws + WS_KVT);
  u16* xT = (u16*)(ws + WS_XT);
  u16* x1T = (u16*)(ws + WS_X1T);
  u16* apT = (u16*)(ws + WS_APT);
  u16* hhT = (u16*)(ws + WS_HHT);
  float* modp = (float*)(ws + WS_MODP);
  f32x2* st1 = (f32x2*)(ws + WS_ST1);
  float* modf = (float*)(ws + WS_MOD);
  f32x2* st2 = (f32x2*)(ws + WS_ST2);
  float* c12 = (float*)(ws + WS_C12);

  (void)hipFuncSetAttribute((const void*)k1_conv, hipFuncAttributeMaxDynamicSharedMemorySize, K1_LDS);
  (void)hipFuncSetAttribute((const void*)g1_gemm, hipFuncAttributeMaxDynamicSharedMemorySize, G1_LDS);
  (void)hipFuncSetAttribute((const void*)g2_gemm, hipFuncAttributeMaxDynamicSharedMemorySize, G2_LDS);

  k0_prep<<<K0_TRB + K0_MODB + K0_STB, 256, 0, stream>>>(x, k, v, shk, c, wmod, kvT, xT, st1, modp);
  k1_conv<<<256, 256, K1_LDS, stream>>>(ssmk, ssmD, kvT, xT, st1, modp, x1T, apT, modf);
  k2_aux<<<512, 256, 0, stream>>>(x1T, st1, modf, w1, w3, st2, c12);
  g1_gemm<<<256, 512, G1_LDS, stream>>>(apT, w1, w3, st2, c12, hhT);
  g2_gemm<<<256, 256, G2_LDS, stream>>>(hhT, w2, x1T, modf, (u16*)d_out);
}
```

```cpp
#include <hip/hip_runtime.h>
#include <hip/hip_bf16.h>
#include <stdint.h>

typedef unsigned short u16;
typedef unsigned int u32;
typedef __attribute__((ext_vector_type(8))) __bf16 bf16x8;
typedef __attribute__((ext_vector_type(2))) __bf16 bf16x2;
typedef __attribute__((ext_vector_type(4))) short s16x4;
typedef __attribute__((ext_vector_type(8))) short s16x8;
typedef __attribute__((ext_vector_type(4))) float f32x4;
typedef __attribute__((ext_vector_type(2))) float f32x2;
typedef __attribute__((ext_vector_type(4))) u32 u32x4;
typedef __attribute__((ext_vector_type(2))) u32 u32x2;

#define S_ 2048
#define D_ 1024
#define F_ 3584

#define WS_KVT (0u)
#define WS_XT (4u << 20)
#define WS_X1T (8u << 20)
#define WS_APT (12u << 20)
#define WS_HHT (16u << 20)
#define WS_MODP (32u << 20)
#define WS_ST1 (33u << 20)
#define WS_MOD ((33u << 20) + (64u << 10))
#define WS_ST2 ((33u << 20) + (128u << 10))
#define WS_C12 ((33u << 20) + (192u << 10))

__device__ __forceinline__ float bflo(u32 u) { return __uint_as_float(u << 16); }
__device__ __forceinline__ float bfhi(u32 u) { return __uint_as_float(u & 0xffff0000u); }
__device__ __forceinline__ float bf1(u16 h) { return __uint_as_float(((u32)h) << 16); }
__device__ __forceinline__ u32 pk2(float a, float b) {
  bf16x2 r = {(__bf16)a, (__bf16)b};
  return __builtin_bit_cast(u32, r);
}
__device__ __forceinline__ float rbf(float a) {
  return __uint_as_float(((u32)__builtin_bit_cast(u16, (__bf16)a)) << 16);
}
__device__ __forceinline__ float silu_f(float g) { return g * __builtin_amdgcn_rcpf(1.0f + __expf(-g)); }

#define GLDS16(gptr, ldsptr)                                                        \
  __builtin_amdgcn_global_load_lds((const __attribute__((address_space(1))) u32*)(gptr), \
                                   (__attribute__((address_space(3))) u32*)(ldsptr), 16, 0, 0)
#define WAIT_V(n) asm volatile("s_waitcnt vmcnt(" #n ")" ::: "memory")
#define WAIT_L(n) asm volatile("s_waitcnt lgkmcnt(" #n ")" ::: "memory")
#define BAR __builtin_amdgcn_s_barrier()
#define SCHED __builtin_amdgcn_sched_barrier(0)

#define TS(k, i)
#define TSC(k, i)
#define TSW(k, i, wv)

__device__ __forceinline__ float wave_sum(float v) {
#pragma unroll
  for (int o = 32; o > 0; o >>= 1) v += __shfl_xor(v, o, 64);
  return v;
}

#define K0_MODB 384
#define K0_STB 256
#define K0_TRB 256
__global__ __launch_bounds__(256) void k0_prep(const u16* __restrict__ x, const u16* __restrict__ kk,
                                               const u16* __restrict__ vv, const u16* __restrict__ shk,
                                               const u16* __restrict__ cvec, const u16* __restrict__ wmod,
                                               u16* __restrict__ kvT, u16* __restrict__ xT,
                                               f32x2* __restrict__ st1, float* __restrict__ modp,
                                               f32x2* __restrict__ st2) {
  __shared__ float red[8 * 256];
  const int tid = threadIdx.x;
  int b = blockIdx.x;
  TS(0, 0);
  if (b < K0_TRB) {
    const int dt = b & 15, stile = b >> 4;
    const int lane = tid & 63, w = tid >> 6;
    const int c8 = lane & 7, sq = lane >> 3;
    const int s0 = stile * 128 + w * 32 + sq * 4;
    const int d0 = dt * 64 + c8 * 8;
    u32x4 kr[7], vr[4], xr[4], sw[4];
#pragma unroll
    for (int i = 0; i < 7; ++i) {
      int s = s0 - 3 + i;
      int sc = s < 0 ? 0 : s;
      kr[i] = *(const u32x4*)(kk + (size_t)sc * D_ + d0);
    }
#pragma unroll
    for (int i = 0; i < 4; ++i) vr[i] = *(const u32x4*)(vv + (size_t)(s0 + i) * D_ + d0);
#pragma unroll
    for (int i = 0; i < 4; ++i) xr[i] = *(const u32x4*)(x + (size_t)(s0 + i) * D_ + d0);
#pragma unroll
    for (int i = 0; i < 4; ++i) sw[i] = *(const u32x4*)(shk + (size_t)d0 * 4 + i * 8);
    if (s0 == 0) {
#pragma unroll
      for (int i = 0; i < 3; ++i) kr[i] = (u32x4){0, 0, 0, 0};
    }
#pragma unroll
    for (int j = 0; j < 8; ++j) {
      u32 wa = sw[j >> 1][(j & 1) * 2], wb = sw[j >> 1][(j & 1) * 2 + 1];
      float w0 = bflo(wa), w1 = bfhi(wa), w2 = bflo(wb), w3 = bfhi(wb);
      float kf[7];
#pragma unroll
      for (int i = 0; i < 7; ++i) kf[i] = (j & 1) ? bfhi(kr[i][j >> 1]) : bflo(kr[i][j >> 1]);
      float o[4];
#pragma unroll
      for (int i = 0; i < 4; ++i) {
        float ks = kf[i] * w3;
        ks = fmaf(kf[i + 1], w2, ks);
        ks = fmaf(kf[i + 2], w1, ks);
        ks = fmaf(kf[i + 3], w0, ks);
        float vf = (j & 1) ? bfhi(vr[i][j >> 1]) : bflo(vr[i][j >> 1]);
        o[i] = ks * vf;
      }
      u32x2 ov = {pk2(o[0], o[1]), pk2(o[2], o[3])};
      *(u32x2*)(kvT + (size_t)(d0 + j) * S_ + s0) = ov;
      const u32 sel = (j & 1) ? 0x07060302u : 0x05040100u;
      u32x2 xv = {__builtin_amdgcn_perm(xr[1][j >> 1], xr[0][j >> 1], sel),
                  __builtin_amdgcn_perm(xr[3][j >> 1], xr[2][j >> 1], sel)};
      *(u32x2*)(xT + (size_t)(d0 + j) * S_ + s0) = xv;
    }
    TS(0, 3);
    return;
  }
  b -= K0_TRB;
  if (b < K0_MODB) {
    const int ks = b / 24, strip = b % 24;
    const int cl = tid & 31, rg = tid >> 5;
    u32x4 wv[8];
    u16 cv[8];
#pragma unroll
    for (int i = 0; i < 8; ++i) {
      int row = ks * 64 + rg + 8 * i;
      wv[i] = *(const u32x4*)(wmod + (size_t)row * (6 * D_) + strip * 256 + cl * 8);
      cv[i] = cvec[row];
    }
    float acc[8];
#pragma unroll
    for (int j = 0; j < 8; ++j) acc[j] = 0.f;
#pragma unroll
    for (int i = 0; i < 8; ++i) {
      float cf = bf1(cv[i]);
      float sc = rbf(cf / (1.0f + __expf(-cf)));
#pragma unroll
      for (int j = 0; j < 4; ++j) {
        acc[2 * j] = fmaf(sc, bflo(wv[i][j]), acc[2 * j]);
        acc[2 * j + 1] = fmaf(sc, bfhi(wv[i][j]), acc[2 * j + 1]);
      }
    }
    *(f32x4*)&red[rg * 256 + cl * 8] = (f32x4){acc[0], acc[1], acc[2], acc[3]};
    *(f32x4*)&red[rg * 256 + cl * 8 + 4] = (f32x4){acc[4], acc[5], acc[6], acc[7]};
    __syncthreads();
    float s = 0.f;
#pragma unroll
    for (int i = 0; i < 8; ++i) s += red[i * 256 + tid];
    modp[ks * (6 * D_) + strip * 256 + tid] = s;
    TS(0, 3);
    return;
  }
  b -= K0_MODB;
  {
    const int lane = tid & 63, w = tid >> 6;
    const int r0 = b * 8 + w * 2;
    u32x4 a[2][2];
#pragma unroll
    for (int r = 0; r < 2; ++r)
#pragma unroll
      for (int h = 0; h < 2; ++h) a[r][h] = *(const u32x4*)(x + (size_t)(r0 + r) * D_ + h * 512 + lane * 8);
#pragma unroll
    for (int r = 0; r < 2; ++r) {
      float s = 0.f;
#pragma unroll
      for (int h = 0; h < 2; ++h)
#pragma unroll
        for (int j = 0; j < 4; ++j) s += bflo(a[r][h][j]) + bfhi(a[r][h][j]);
      s = wave_sum(s);
      float m = s * (1.0f / D_);
      float q = 0.f;
#pragma unroll
      for (int h = 0; h < 2; ++h)
#pragma unroll
        for (int j = 0; j < 4; ++j) {
          float d0 = bflo(a[r][h][j]) - m, d1 = bfhi(a[r][h][j]) - m;
          q = fmaf(d0, d0, q);
          q = fmaf(d1, d1, q);
        }
      q = wave_sum(q);
      float rs = rsqrtf(q * (1.0f / D_) + 1e-5f);
      if (lane == 0) {
        st1[r0 + r] = (f32x2){m, rs};
        st2[r0 + r] = (f32x2){0.f, 0.f};
      }
    }
    TS(0, 3);
  }
}

#define K1_KOFF 0
#define K1_UOFF 4224
#define K1_XOFF 8576
#define K1_YOFF 12672
#define K1_COFF 20864
#define K1_WSZ 21408
#define K1_STOFF (4 * K1_WSZ)
#define K1_MVOFF (K1_STOFF + 16384)
#define K1_REDOFF (K1_MVOFF + 1024)
#define K1_LDS (K1_REDOFF + 8 * 448 * 4)

typedef __attribute__((address_space(3))) s16x4* lds_s16x4p;
__device__ __forceinline__ bf16x8 tr_frag(const char* p0, const char* p1) {
  s16x4 a = __builtin_amdgcn_ds_read_tr16_b64_v4i16((lds_s16x4p)p0);
  s16x4 b = __builtin_amdgcn_ds_read_tr16_b64_v4i16((lds_s16x4p)p1);
  s16x8 c = __builtin_shufflevector(a, b, 0, 1, 2, 3, 4, 5, 6, 7);
  return __builtin_bit_cast(bf16x8, c);
}
template <int CTRL>
__device__ __forceinline__ float dppz(float v) {
  return __builtin_bit_cast(float, __builtin_amdgcn_update_dpp(0, __builtin_bit_cast(int, v), CTRL, 0xf, 0xf, true));
}
#define SYNC_ALL asm volatile("s_waitcnt vmcnt(0) lgkmcnt(0)\n\ts_barrier" ::: "memory")
#define SYNC_L asm volatile("s_waitcnt lgkmcnt(0)\n\ts_barrier" ::: "memory")

__global__ __launch_bounds__(768, 3) void k1_conv(const u16* __restrict__ ssmk, const u16* __restrict__ ssmD,
                                                  const u16* __restrict__ kvT, const u16* __restrict__ xT,
                                                  const f32x2* __restrict__ st1, const float* __restrict__ modp,
                                                  const u16* __restrict__ w1, const u16* __restrict__ w3,
                                                  u16* __restrict__ x1T, u16* __restrict__ apT,
                                                  float* __restrict__ modf, float* __restrict__ c12) {
  extern __shared__ __attribute__((aligned(16))) char smem[];
  const int tid = threadIdx.x, lane = tid & 63;
  const int w = __builtin_amdgcn_readfirstlane(tid >> 6);
  TS(1, 0);
  if (w >= 8) {
    const int at = tid - 512;
    const int b = blockIdx.x;
    const int nt = (b & 7) * 4 + ((b >> 3) & 3), ks = b >> 5;
    float mp[16];
    {
      const u32 po = ((3 + (at >> 7)) * D_ + ks * 128 + (at & 127)) * 4;
#pragma unroll
      for (int i = 0; i < 16; ++i)
        asm volatile("global_load_dword %0, %1, %2" : "=v"(mp[i]) : "v"(po), "s"(modp + i * (6 * D_)) : "memory");
    }
    SCHED;
    const int ac = at < 224 ? at : 223;
    const int cu = ac % 28, rg = ac / 28;
    u32x4 wv[16];
    {
      const u16* wp = (cu < 14 ? w1 : w3) + (size_t)(ks * 128 + rg) * F_ + nt * 112 + (cu % 14) * 8;
#pragma unroll
      for (int j = 0; j < 16; ++j)
        asm volatile("global_load_dwordx4 %0, %1, off" : "=v"(wv[j]) : "v"(wp + (size_t)(8 * j) * F_) : "memory");
    }
    SCHED;
    asm volatile("s_waitcnt vmcnt(16)"
                 : "+v"(mp[0]), "+v"(mp[1]), "+v"(mp[2]), "+v"(mp[3]), "+v"(mp[4]), "+v"(mp[5]), "+v"(mp[6]),
                   "+v"(mp[7]), "+v"(mp[8]), "+v"(mp[9]), "+v"(mp[10]), "+v"(mp[11]), "+v"(mp[12]), "+v"(mp[13]),
                   "+v"(mp[14]), "+v"(mp[15])
                 :
                 : "memory");
    {
      float s = 0.f;
#pragma unroll
      for (int i = 0; i < 16; ++i) s += mp[i];
      s = rbf(s);
      if (at >= 128) s = rbf(1.0f + s);
      ((float*)(smem + K1_MVOFF))[(at & 127) * 2 + (at >> 7)] = s;
    }
    SYNC_L;
    float a1[8], a2[8];
#pragma unroll
    for (int j = 0; j < 8; ++j) { a1[j] = 0.f; a2[j] = 0.f; }
#pragma unroll
    for (int j = 0; j < 16; ++j) {
      f32x2 mv = *(const f32x2*)(smem + K1_MVOFF + (rg + 8 * j) * 8);
      asm volatile("s_waitcnt vmcnt(%1)" : "+v"(wv[j]) : "i"(15 - j));
#pragma unroll
      for (int e = 0; e < 4; ++e) {
        float lo = bflo(wv[j][e]), hi = bfhi(wv[j][e]);
        a1[2 * e] = fmaf(mv[1], lo, a1[2 * e]); a1[2 * e + 1] = fmaf(mv[1], hi, a1[2 * e + 1]);
        a2[2 * e] = fmaf(mv[0], lo, a2[2 * e]); a2[2 * e + 1] = fmaf(mv[0], hi, a2[2 * e + 1]);
      }
    }
    if (at < 224) {
      float* red = (float*)(smem + K1_REDOFF) + rg * 448 + cu * 8;
      *(f32x4*)(red) = (f32x4){a1[0], a1[1], a1[2], a1[3]};
      *(f32x4*)(red + 4) = (f32x4){a1[4], a1[5], a1[6], a1[7]};
      *(f32x4*)(red + 224) = (f32x4){a2[0], a2[1], a2[2], a2[3]};
      *(f32x4*)(red + 228) = (f32x4){a2[4], a2[5], a2[6], a2[7]};
    }
    SYNC_L;
    if (at < 224) {
      const f32x2* red = (const f32x2*)(smem + K1_REDOFF) + at;
      f32x2 s = red[0];
#pragma unroll
      for (int g = 1; g < 8; ++g) s += red[g * 224];
      *(f32x2*)(c12 + (ks * 32 + nt) * 448 + at * 2) = s;
    }
    return;
  }
  const int c = w & 3, h = w >> 2;
  const int ch = blockIdx.x * 4 + c;
  char* wb = smem + c * K1_WSZ;
  if (h == 0) {
    if (lane < 8) *(u32x4*)(wb + K1_KOFF + lane * 16) = (u32x4){0, 0, 0, 0};
    else if (lane == 8) *(u32x4*)(wb + K1_COFF) = (u32x4){0, 0, 0, 0};
  } else {
    if (lane < 16) *(u32x4*)(wb + K1_UOFF + lane * 16) = (u32x4){0, 0, 0, 0};
  }
  float mv0, mv1;
  u32 ddu;
  {
    const u32 o0 = ((lane & 15) * (6 * D_) + (lane >> 4) * D_) * 4;
    const u32 o1 = ((lane & 15) * (6 * D_) + (4 + ((lane >> 4) & 1)) * D_) * 4;
    asm volatile("global_load_dword %0, %1, %2" : "=v"(mv0) : "v"(o0), "s"(modp + ch) : "memory");
    asm volatile("global_load_dword %0, %1, %2" : "=v"(mv1) : "v"(o1), "s"(modp + ch) : "memory");
    asm volatile("global_load_ushort %0, %1, %2" : "=v"(ddu) : "v"(0u), "s"(ssmD + ch) : "memory");
  }
  SCHED;
  {
    const u16* src = (h ? kvT : ssmk) + (size_t)ch * S_ + lane * 8;
    char* dst = wb + (h ? K1_UOFF + 256 : K1_KOFF + 128);
#pragma unroll
    for (int i = 0; i < 4; ++i) GLDS16(src + i * 512, dst + i * 1024);
#pragma unroll
    for (int i = 0; i < 2; ++i) {
      GLDS16(xT + (size_t)ch * S_ + (2 * h + i) * 512 + lane * 8, wb + K1_XOFF + (2 * h + i) * 1024);
      GLDS16((const char*)st1 + (w * 2 + i) * 1024 + lane * 16, smem + K1_STOFF + (w * 2 + i) * 1024);
    }
  }
  SCHED;
  asm volatile("s_waitcnt vmcnt(8)" : "+v"(mv0), "+v"(mv1), "+v"(ddu) : : "memory");
  const float dd = bflo(ddu);
#pragma unroll
  for (int o = 8; o > 0; o >>= 1) {
    mv0 += __shfl_xor(mv0, o, 64);
    mv1 += __shfl_xor(mv1, o, 64);
  }
  mv0 = rbf(mv0);
  mv1 = rbf(mv1);
  const float sa = __shfl(mv0, 0, 64), ca1 = rbf(1.0f + __shfl(mv0, 16, 64)), ga = __shfl(mv0, 32, 64);
  const float cb1 = rbf(1.0f + __shfl(mv1, 0, 64));
  if (lane == 16 && h == 0) modf[ch] = mv1;
  SYNC_ALL;
  TS(1, 1);

  const int G = lane >> 4, q = (lane >> 2) & 3, p = lane & 3;
  const char* abase = wb + K1_KOFF + 128 - 96 + 32 * p + 8 * (8 * G + q);
  const char* bbase = wb + K1_UOFF + 256 + 96 + 8 * p - 8 * (8 * G + q) + 128 * h;

  bf16x8 Af[16], Bf[16];
  float* yb = (float*)(wb + K1_YOFF) + 64 * h + lane;
  float* cy = (float*)(wb + K1_COFF) + 4 * (h + 1);
#pragma unroll
  for (int a = 0; a < 16; ++a) {
    Af[a] = tr_frag(abase + 256 * a, abase + 256 * a + 32);
    Bf[a] = tr_frag(bbase + 256 * a, bbase + 256 * a - 32);
    f32x4 z = {0, 0, 0, 0};
#pragma unroll
    for (int i = 0; i <= a; ++i) z = __builtin_amdgcn_mfma_f32_16x16x32_bf16(Af[i], Bf[a - i], z, 0, 0, 0);
    float w2 = dppz<0x138>(z[3]) + z[2];
    float w1 = dppz<0x138>(w2) + z[1];
    float y = dppz<0x138>(w1) + z[0];
    yb[128 * a] = y;
    if (lane == 63) *(f32x4*)(cy + 8 * a) = (f32x4){w1, w2, z[3], 0.f};
  }
  SYNC_L;
  TS(1, 2);
#pragma unroll
  for (int it = 0; it < 2; ++it) {
    const int so = h * 1024 + it * 512 + lane * 8;
    f32x4 y0 = *(const f32x4*)(wb + K1_YOFF + so * 4);
    f32x4 y1 = *(const f32x4*)(wb + K1_YOFF + so * 4 + 16);
    f32x4 cv = *(const f32x4*)(wb + K1_COFF + ((lane & 7) ? 0 : (so >> 6) * 16));
    u32x4 uv = *(const u32x4*)(wb + K1_UOFF + 256 + so * 2);
    u32x4 xv = *(const u32x4*)(wb + K1_XOFF + so * 2);
    y0 += cv;
    u32x4 o1, o2;
#pragma unroll
    for (int j = 0; j < 4; ++j) {
      f32x4 st = *(const f32x4*)(smem + K1_STOFF + (so + 2 * j) * 8);
      float r[2], ap[2];
#pragma unroll
      for (int e = 0; e < 2; ++e) {
        float xf = e ? bfhi(xv[j]) : bflo(xv[j]);
        float yf = j < 2 ? y0[2 * j + e] : y1[2 * j - 4 + e];
        float uf = e ? bfhi(uv[j]) : bflo(uv[j]);
        float m1 = st[2 * e], r1 = st[2 * e + 1];
        float h1 = fmaf((xf - m1) * r1, ca1, sa);
        float yy = fmaf(uf, dd, yf);
        r[e] = fmaf(ga, h1 * yy, xf);
      }
      o1[j] = pk2(r[0], r[1]);
      ap[0] = (bflo(o1[j]) - st[0]) * cb1;
      ap[1] = (bfhi(o1[j]) - st[2]) * cb1;
      o2[j] = pk2(ap[0], ap[1]);
    }
    *(u32x4*)(x1T + (size_t)ch * S_ + so) = o1;
    *(u32x4*)(apT + (size_t)ch * S_ + so) = o2;
  }
  TS(1, 3);
}

#define G1_SLOT 32768
#define G1_R 5
#define G1_BOFF 16384
#define G1_LDS (G1_R * G1_SLOT)

#define TRRD(dst, addr, off) \
  asm volatile("ds_read_b64_tr_b16 %0, %1 offset:%2" : "=v"(dst) : "v"(addr), "i"(off))

typedef __attribute__((address_space(1))) unsigned long long gu64;

__device__ __forceinline__ void wf_merge(float& m, float& q, float ma, float qa, float mb, float qb, float hn) {
  float d = mb - ma;
  m = 0.5f * (ma + mb);
  q = fmaf(d * hn, d, qa + qb);
}
__device__ __forceinline__ void wf_swap32(float& m, float& q, float ma, float qa, float mb, float qb, float hn) {
  auto a = __builtin_amdgcn_permlane32_swap(__float_as_uint(ma), __float_as_uint(mb), false, false);
  auto c = __builtin_amdgcn_permlane32_swap(__float_as_uint(qa), __float_as_uint(qb), false, false);
  wf_merge(m, q, __uint_as_float(a[0]), __uint_as_float(c[0]), __uint_as_float(a[1]), __uint_as_float(c[1]), hn);
}
__device__ __forceinline__ void wf_swap16(float& m, float& q, float ma, float qa, float mb, float qb, float hn) {
  auto a = __builtin_amdgcn_permlane16_swap(__float_as_uint(ma), __float_as_uint(mb), false, false);
  auto c = __builtin_amdgcn_permlane16_swap(__float_as_uint(qa), __float_as_uint(qb), false, false);
  wf_merge(m, q, __uint_as_float(a[0]), __uint_as_float(c[0]), __uint_as_float(a[1]), __uint_as_float(c[1]), hn);
}
template <int CTRL>
__device__ __forceinline__ void wf_dpp(float& m, float& q, float hn) {
  float mo = dppz<CTRL>(m), qo = dppz<CTRL>(q);
  wf_merge(m, q, m, q, mo, qo, hn);
}

__global__ __launch_bounds__(512, 2) void g1_gemm(const u16* __restrict__ apT, const u16* __restrict__ w1,
                                                  const u16* __restrict__ w3, const u16* __restrict__ x1T,
                                                  const f32x2* __restrict__ st1, unsigned long long* st2,
                                                  const float* __restrict__ c12, const u16* __restrict__ w2,
                                                  u16* __restrict__ hh) {
  extern __shared__ __attribute__((aligned(16))) char smem[];
  const int tid = threadIdx.x, lane = tid & 63;
  const int w = __builtin_amdgcn_readfirstlane(tid >> 6);
  const int grp = w >> 2;
  const int wm = w & 3, wn = grp;
  const int b = blockIdx.x;
  const int xcd = b & 7, idx = b >> 3;
  const int mt = idx & 7, nt = xcd * 4 + (idx >> 3);
  const int s0 = mt * 256, f0 = nt * 112;
  TS(3, 0);

  const int r0 = (xcd * 32 + idx) * 8;
  u32x4 xa, xb;
  float m1v, cp[8];
  {
    const u32 xo = tid * (S_ * 2);
    const char* xbase0 = (const char*)x1T + r0 * 2;
    const char* xbase1 = xbase0 + 512 * (S_ * 2);
    asm volatile("global_load_dwordx4 %0, %1, %2" : "=v"(xa) : "v"(xo), "s"(xbase0) : "memory");
    asm volatile("global_load_dwordx4 %0, %1, %2" : "=v"(xb) : "v"(xo), "s"(xbase1) : "memory");
    const u32 mo = (lane & 7) * 8;
    asm volatile("global_load_dword %0, %1, %2" : "=v"(m1v) : "v"(mo), "s"(st1 + r0) : "memory");
    const u32 co = (tid < 448 ? tid : 447) * 4;
#pragma unroll
    for (int ks = 0; ks < 8; ++ks)
      asm volatile("global_load_dword %0, %1, %2" : "=v"(cp[ks]) : "v"(co), "s"(c12 + (ks * 32 + nt) * 448) : "memory");
  }
  SCHED;

  const char* asrc[2];
  const char* bsrc[2];
#pragma unroll
  for (int j = 0; j < 2; ++j) {
    int row = 4 * w + 2 * j + (lane >> 5);
    int c = (lane & 31) ^ ((row & 7) << 1);
    asrc[j] = (const char*)apT + (size_t)row * (S_ * 2) + s0 * 2 + c * 16;
    int xx = j * 64 + lane;
    if (xx > 111) xx = 111;
    int brow = 4 * w + xx / 28;
    int u = (xx % 28) ^ (3 * (w & 1));
    int fu = (u >> 2) * 2 + (u & 1);
    bsrc[j] = (const char*)((u & 2) ? w3 : w1) + (size_t)brow * (F_ * 2) + f0 * 2 + fu * 16;
  }
  const int adst = w * 2048, bdst = G1_BOFF + w * 2048;
#define G1_STAGE(slot)                                                   \
  do {                                                                   \
    char* sb_ = smem + (slot) * G1_SLOT;                                 \
    GLDS16(asrc[0], sb_ + adst);                                         \
    GLDS16(asrc[1], sb_ + adst + 1024);                                  \
    GLDS16(bsrc[0], sb_ + bdst);                                         \
    GLDS16(bsrc[1], sb_ + bdst + 1024);                                  \
    asrc[0] += 32 * S_ * 2; asrc[1] += 32 * S_ * 2;                      \
    bsrc[0] += 32 * F_ * 2; bsrc[1] += 32 * F_ * 2;                      \
  } while (0)

#pragma unroll
  for (int i = 0; i < G1_R - 1; ++i) G1_STAGE(i);
  SCHED;

  const int g = lane >> 4, q = (lane >> 2) & 3, p = lane & 3;
  const u32 lds0 = (u32)(uintptr_t)(__attribute__((address_space(3))) char*)smem;
  u32 ard[4], brd[3];
  {
    const int row = 4 * g + q;
#pragma unroll
    for (int m = 0; m < 4; ++m) {
      int c = (8 * wm + 2 * m + (p >> 1)) ^ ((row & 7) << 1);
      ard[m] = lds0 + row * 512 + c * 16 + 8 * (p & 1);
    }
#pragma unroll
    for (int n = 0; n < 3; ++n) {
      int fu = 7 * wn + (n < 2 ? (p >> 1) : 6);
      int mat = n < 2 ? n : (p >> 1);
      int u = (4 * (fu >> 1) + 2 * mat + (fu & 1)) ^ (3 * (g & 1));
      brd[n] = lds0 + G1_BOFF + g * 2048 + q * 448 + u * 16 + 8 * (p & 1);
    }
  }

  asm volatile("s_waitcnt vmcnt(16)"
               : "+v"(xa), "+v"(xb), "+v"(m1v), "+v"(cp[0]), "+v"(cp[1]), "+v"(cp[2]), "+v"(cp[3]), "+v"(cp[4]),
                 "+v"(cp[5]), "+v"(cp[6]), "+v"(cp[7])
               :
               : "memory");
  float csum = ((cp[0] + cp[1]) + (cp[2] + cp[3])) + ((cp[4] + cp[5]) + (cp[6] + cp[7]));
  asm volatile("" : "+v"(csum));
  {
    float sm[8], sq[8];
#pragma unroll
    for (int j = 0; j < 4; ++j) {
      wf_merge(sm[2 * j], sq[2 * j], bflo(xa[j]), 0.f, bflo(xb[j]), 0.f, 0.5f);
      wf_merge(sm[2 * j + 1], sq[2 * j + 1], bfhi(xa[j]), 0.f, bfhi(xb[j]), 0.f, 0.5f);
    }
#pragma unroll
    for (int j = 0; j < 4; ++j) wf_swap32(sm[j], sq[j], sm[j], sq[j], sm[j + 4], sq[j + 4], 1.f);
#pragma unroll
    for (int j = 0; j < 2; ++j) wf_swap16(sm[j], sq[j], sm[j], sq[j], sm[j + 2], sq[j + 2], 2.f);
    const bool hi8 = lane & 8;
    float mo = dppz<0x128>(hi8 ? sm[0] : sm[1]), qo = dppz<0x128>(hi8 ? sq[0] : sq[1]);
    float mm, qq;
    wf_merge(mm, qq, hi8 ? sm[1] : sm[0], hi8 ? sq[1] : sq[0], mo, qo, 4.f);
    wf_dpp<0xB1>(mm, qq, 8.f);
    wf_dpp<0x4E>(mm, qq, 16.f);
    wf_dpp<0x141>(mm, qq, 32.f);
    if ((lane & 7) == 0) {
      u32x2 v = {__float_as_uint(mm), __float_as_uint(qq)};
      u32 ad = lds0 + 4 * G1_SLOT + w * 64 + lane;
      asm volatile("ds_write_b64 %0, %1" ::"v"(ad), "v"(v) : "memory");
    }
  }

  f32x4 acc[7][4];
#pragma unroll
  for (int m = 0; m < 4; ++m)
#pragma unroll
    for (int n = 0; n < 7; ++n) acc[n][m] = (f32x4){0, 0, 0, 0};

  WAIT_L(0);
  WAIT_V(12);
  BAR;
  if (w == 0) {
    u32x2 v;
    u32 ad = lds0 + 4 * G1_SLOT + lane * 8;
    asm volatile("ds_read_b64 %0, %1\n\ts_waitcnt lgkmcnt(0)" : "=v"(v) : "v"(ad) : "memory");
    float mm = __uint_as_float(v[0]), qq = __uint_as_float(v[1]);
    wf_dpp<0x128>(mm, qq, 64.f);
    wf_swap16(mm, qq, mm, qq, mm, qq, 128.f);
    wf_swap32(mm, qq, mm, qq, mm, qq, 256.f);
    float r2 = rsqrtf(qq * (1.0f / D_) + 1e-5f);
    float rd = r2 * (mm - m1v);
    u32 rb = __float_as_uint(r2);
    if (rb == 0u) rb = 0x80000000u;
    if (lane < 8)
      __hip_atomic_store((gu64*)(st2 + r0 + lane), ((unsigned long long)__float_as_uint(rd) << 32) | rb,
                         __ATOMIC_RELAXED, __HIP_MEMORY_SCOPE_AGENT);
  }
  if (grp == 1) BAR;
  TS(3, 1);
  TSC(5, 0);

  int rslot = 0;
  int wslot = G1_R - 1;
#define G1_ITER(STMT, WV)                                                                     \
  do {                                                                                        \
    u32x2 af[4][2], bf[7][2];                                                                 \
    const u32 so = rslot;                                                                     \
    _Pragma("unroll") for (int n = 0; n < 7; ++n) {                                           \
      u32 ad = brd[n < 6 ? (n & 1) : 2] + so;                                                 \
      TRRD(bf[n][0], ad, (n < 6 ? (n >> 1) * 64 : 0));                                        \
      TRRD(bf[n][1], ad, (n < 6 ? (n >> 1) * 64 : 0) + 8192);                                 \
    }                                                                                         \
    _Pragma("unroll") for (int m = 0; m < 4; ++m) {                                           \
      u32 ad = ard[m] + so;                                                                   \
      TRRD(af[m][0], ad, 0);                                                                  \
      TRRD(af[m][1], ad, 8192);                                                               \
    }                                                                                         \
    STMT;                                                                                     \
    WAIT_L(0);                                                                                \
    if (grp == 1) WV;                                                                         \
    rslot += G1_SLOT; if (rslot == G1_R * G1_SLOT) rslot = 0;                                 \
    wslot += 1; if (wslot == G1_R) wslot = 0;                                                 \
    SCHED;                                                                                    \
    BAR;                                                                                      \
    SCHED;                                                                                    \
    __builtin_amdgcn_s_setprio(1);                                                            \
    _Pragma("unroll") for (int m = 0; m < 4; ++m) {                                           \
      u32x4 a4 = {af[m][0][0], af[m][0][1], af[m][1][0], af[m][1][1]};                        \
      _Pragma("unroll") for (int n = 0; n < 7; ++n) {                                         \
        u32x4 b4 = {bf[n][0][0], bf[n][0][1], bf[n][1][0], bf[n][1][1]};                      \
        acc[n][m] = __builtin_amdgcn_mfma_f32_16x16x32_bf16(                                  \
            __builtin_bit_cast(bf16x8, b4), __builtin_bit_cast(bf16x8, a4), acc[n][m], 0, 0, 0); \
      }                                                                                       \
    }                                                                                         \
    __builtin_amdgcn_s_setprio(0);                                                            \
    if (grp == 0) WV;                                                                         \
    SCHED;                                                                                    \
    BAR;                                                                                      \
    SCHED;                                                                                    \
  } while (0)

  const char* pf = (const char*)w2 + (size_t)b * 28672 + (w < 7 ? w : 6) * 4096 + lane * 64;
  for (int t = 0; t < 28; ++t) {
    G1_ITER({
      G1_STAGE(wslot);
      if (t == 2)
        __builtin_amdgcn_global_load_lds((const __attribute__((address_space(1))) u32*)pf,
                                         (__attribute__((address_space(3))) u32*)(smem + wslot * G1_SLOT + bdst + 1792),
                                         4, 0, 0);
    }, WAIT_V(12));
  }
  unsigned long long sg[4];
  gu64* sp = (gu64*)(st2 + s0 + wm * 64 + (lane & 15));
#pragma unroll
  for (int m = 0; m < 4; ++m)
    asm volatile("global_load_dwordx2 %0, %1, off offset:%2 sc1" : "=v"(sg[m]) : "v"(sp), "i"(128 * m) : "memory");
  SCHED;
  G1_ITER({
    if (tid < 448) {
      u32 ad = lds0 + 2 * G1_SLOT + tid * 4;
      asm volatile("ds_write_b32 %0, %1" ::"v"(ad), "v"(csum) : "memory");
    }
  }, WAIT_V(12));
  G1_ITER({}, WAIT_V(8));
  G1_ITER({}, WAIT_V(4));
  G1_ITER({}, {});
  if (grp == 0) BAR;
  TS(3, 2);
  TSC(5, 1);

  {
    asm volatile("s_waitcnt vmcnt(0)" : "+v"(sg[0]), "+v"(sg[1]), "+v"(sg[2]), "+v"(sg[3]) : : "memory");
    for (int spin = 0; spin < (1 << 22); ++spin) {
      bool ok = ((u32)sg[0] != 0u) & ((u32)sg[1] != 0u) & ((u32)sg[2] != 0u) & ((u32)sg[3] != 0u);
      if (__all(ok)) break;
      __builtin_amdgcn_s_sleep(8);
#pragma unroll
      for (int m = 0; m < 4; ++m) sg[m] = __hip_atomic_load(sp + 16 * m, __ATOMIC_RELAXED, __HIP_MEMORY_SCOPE_AGENT);
    }
    float r2[4], rd[4];
#pragma unroll
    for (int m = 0; m < 4; ++m) {
      r2[m] = __uint_as_float((u32)sg[m]);
      rd[m] = -__uint_as_float((u32)(sg[m] >> 32));
    }
    const float* cl = (const float*)(smem + 2 * G1_SLOT) + wn * 56;
    char* ob = (char*)hh + ((size_t)((s0 + wm * 64) >> 4) * 448 + nt * 14 + wn * 7) * 256 + (lane & 15) * 16;
#pragma unroll
    for (int j = 0; j < 3; ++j) {
      const f32x4 c1g = *(const f32x4*)(cl + 16 * j + 4 * g), c1u = *(const f32x4*)(cl + 112 + 16 * j + 4 * g);
      const f32x4 c2g = *(const f32x4*)(cl + 224 + 16 * j + 4 * g), c2u = *(const f32x4*)(cl + 336 + 16 * j + 4 * g);
      u32 o[4][2];
#pragma unroll
      for (int m = 0; m < 4; ++m) {
        float hv[4];
#pragma unroll
        for (int r = 0; r < 4; ++r) {
          float gt = fmaf(acc[2 * j][m][r], r2[m], fmaf(rd[m], c1g[r], c2g[r]));
          float up = fmaf(acc[2 * j + 1][m][r], r2[m], fmaf(rd[m], c1u[r], c2u[r]));
          hv[r] = silu_f(gt) * up;
        }
        o[m][0] = pk2(hv[0], hv[1]);
        o[m][1] = pk2(hv[2], hv[3]);
      }
#pragma unroll
      for (int mp = 0; mp < 2; ++mp) {
        auto d0 = __builtin_amdgcn_permlane16_swap(o[2 * mp][0], o[2 * mp + 1][0], false, false);
        auto d1 = __builtin_amdgcn_permlane16_swap(o[2 * mp][1], o[2 * mp + 1][1], false, false);
        u32x4 ov = {d0[0], d1[0], d0[1], d1[1]};
        *(u32x4*)(ob + ((size_t)(2 * mp + (g & 1)) * 448 + 2 * j + (g >> 1)) * 256) = ov;
      }
    }
    {
      const int co = (g >> 1) * 112 + 48 + 4 * (g & 1);
      const f32x4 c1 = *(const f32x4*)(cl + co), c2 = *(const f32x4*)(cl + 224 + co);
      u32 o[2][2];
#pragma unroll
      for (int mm = 0; mm < 2; ++mm) {
        float hv[4];
#pragma unroll
        for (int r = 0; r < 4; ++r) {
          float va = fmaf(acc[6][mm][r], r2[mm], fmaf(rd[mm], c1[r], c2[r]));
          float vb = fmaf(acc[6][mm + 2][r], r2[mm + 2], fmaf(rd[mm + 2], c1[r], c2[r]));
          auto sw = __builtin_amdgcn_permlane32_swap(__float_as_uint(va), __float_as_uint(vb), false, false);
          hv[r] = silu_f(__uint_as_float(sw[0])) * __uint_as_float(sw[1]);
        }
        o[mm][0] = pk2(hv[0], hv[1]);
        o[mm][1] = pk2(hv[2], hv[3]);
      }
      auto d0 = __builtin_amdgcn_permlane16_swap(o[0][0], o[1][0], false, false);
      auto d1 = __builtin_amdgcn_permlane16_swap(o[0][1], o[1][1], false, false);
      u32x4 ov = {d0[0], d1[0], d0[1], d1[1]};
      *(u32x4*)(ob + ((size_t)g * 448 + 6) * 256) = ov;
    }
  }
  TS(3, 3);
}

#define G2_BSLOT 4096
#define G2_XOFF 98304
#define G2_LDS (G2_XOFF + 16384)

__global__ __launch_bounds__(256, 1) void g2_gemm(const u16* __restrict__ hh, const u16* __restrict__ w2,
                                                  const u16* __restrict__ x1T, const float* __restrict__ modf,
                                                  u16* __restrict__ out) {
  extern __shared__ __attribute__((aligned(16))) char smem[];
  const int tid = threadIdx.x, lane = tid & 63;
  const int w = __builtin_amdgcn_readfirstlane(tid >> 6);
  const int b = blockIdx.x;
  const int xcd = b & 7, idx = b >> 3;
  const int mt = (xcd >> 1) * 4 + (idx & 3), nt = (xcd & 1) * 8 + (idx >> 2);
  const int s0 = mt * 128, d0 = nt * 64;
  char* wb = smem + w * (3 * G2_BSLOT);
  TS(4, 0);

#pragma unroll
  for (int i = 0; i < 4; ++i)
    GLDS16((const char*)x1T + (size_t)(d0 + 16 * i + (lane >> 2)) * (S_ * 2) + (s0 + 32 * w) * 2 + (lane & 3) * 16,
           smem + G2_XOFF + w * 4096 + i * 1024);

  u32 boff[2];
#pragma unroll
  for (int i = 0; i < 2; ++i) {
    int rho = 8 * i + (lane >> 3);
    int f = 8 * ((rho >> 2) & 3) + (rho & 3);
    boff[i] = (u32)(f * (D_ * 2) + (((lane & 7) ^ (((rho >> 1) & 3) << 1)) * 16));
  }
  const char* bbase = (const char*)w2 + (size_t)(32 * w) * (D_ * 2) + d0 * 2;
  const char* ab[8];
#pragma unroll
  for (int m = 0; m < 8; ++m)
    ab[m] = (const char*)hh + (size_t)((s0 >> 4) + ((m + 2 * w) & 7)) * (448 * 256);
  u32 aoff = w * 1024 + lane * 16;
#define G2_LDA(set, m) \
  asm volatile("global_load_dwordx4 %0, %1, %2" : "=v"(A[set][m]) : "v"(aoff), "s"(ab[m]) : "memory")
#define G2_LDB(slot, i) \
  GLDS16(bbase + (size_t)((i) >> 1) * (4 * D_ * 2) + boff[(i) & 1], wb + (slot) * G2_BSLOT + (i) * 1024)
#define G2_ADV do { aoff += 4096; bbase += (size_t)128 * D_ * 2; } while (0)

  const int g = lane >> 4, q = (lane >> 2) & 3, p = lane & 3;
  const u32 lds0 = (u32)(uintptr_t)(__attribute__((address_space(3))) char*)smem;
  u32 brd[4];
  {
    const int row = 4 * g + q;
#pragma unroll
    for (int n = 0; n < 4; ++n) {
      int c = (2 * n + (p >> 1)) ^ (((row >> 1) & 3) << 1);
      brd[n] = lds0 + w * (3 * G2_BSLOT) + row * 128 + c * 16 + 8 * (p & 1);
    }
  }
  f32x4 acc[4][8];
  u32x4 A[3][8];
  u32x2 bf[2][4][2];
#define G2_PRO(set)                                              \
  do {                                                           \
    _Pragma("unroll") for (int i = 0; i < 4; ++i) G2_LDB(set, i); \
    SCHED;                                                       \
    _Pragma("unroll") for (int m = 0; m < 8; ++m) G2_LDA(set, m); \
    SCHED;                                                       \
    G2_ADV;                                                      \
  } while (0)
#define G2_RDB(bs, slot)                                                   \
  do {                                                                     \
    _Pragma("unroll") for (int n = 0; n < 4; ++n) {                        \
      TRRD(bf[bs][n][0], brd[n], (slot) * G2_BSLOT);                       \
      TRRD(bf[bs][n][1], brd[n], (slot) * G2_BSLOT + 16 * 128);            \
    }                                                                      \
  } while (0)
  G2_PRO(0); G2_PRO(1); G2_PRO(2);
  WAIT_V(32);
  G2_RDB(0, 0);
#define G2_B4(bs, n) ((u32x4){bf[bs][n][0][0], bf[bs][n][0][1], bf[bs][n][1][0], bf[bs][n][1][1]})
#define G2_MFMA4                                          \
  "v_mfma_f32_16x16x32_bf16 %0, %5, %4, %0\n\t"           \
  "v_mfma_f32_16x16x32_bf16 %1, %6, %4, %1\n\t"           \
  "v_mfma_f32_16x16x32_bf16 %2, %7, %4, %2\n\t"           \
  "v_mfma_f32_16x16x32_bf16 %3, %8, %4, %3\n\t"
#define G2_MML(set, bs, m, WV)                                                                       \
  asm volatile("s_waitcnt vmcnt(%11)\n\t" G2_MFMA4 "global_load_dwordx4 %4, %9, %10"                 \
               : "+a"(acc[0][m]), "+a"(acc[1][m]), "+a"(acc[2][m]), "+a"(acc[3][m]), "+v"(A[set][m]) \
               : "v"(G2_B4(bs, 0)), "v"(G2_B4(bs, 1)), "v"(G2_B4(bs, 2)), "v"(G2_B4(bs, 3)),         \
                 "v"(aoff), "s"(ab[m]), "i"(WV)                                                      \
               : "memory")
#define G2_MML0(set, bs, m, WV)                                                                      \
  asm volatile("s_waitcnt vmcnt(%11)\n\t"                                                            \
               "v_mfma_f32_16x16x32_bf16 %0, %5, %4, 0\n\t"                                          \
               "v_mfma_f32_16x16x32_bf16 %1, %6, %4, 0\n\t"                                          \
               "v_mfma_f32_16x16x32_bf16 %2, %7, %4, 0\n\t"                                          \
               "v_mfma_f32_16x16x32_bf16 %3, %8, %4, 0\n\t"                                          \
               "global_load_dwordx4 %4, %9, %10"                                                     \
               : "=a"(acc[0][m]), "=a"(acc[1][m]), "=a"(acc[2][m]), "=a"(acc[3][m]), "+v"(A[set][m]) \
               : "v"(G2_B4(bs, 0)), "v"(G2_B4(bs, 1)), "v"(G2_B4(bs, 2)), "v"(G2_B4(bs, 3)),         \
                 "v"(aoff), "s"(ab[m]), "i"(WV)                                                      \
               : "memory")
#define G2_MM(set, bs, m, WV)                                                                        \
  asm volatile("s_waitcnt vmcnt(%9)\n\t" G2_MFMA4                                                    \
               : "+a"(acc[0][m]), "+a"(acc[1][m]), "+a"(acc[2][m]), "+a"(acc[3][m])                  \
               : "v"(A[set][m]), "v"(G2_B4(bs, 0)), "v"(G2_B4(bs, 1)), "v"(G2_B4(bs, 2)),            \
                 "v"(G2_B4(bs, 3)), "i"(WV)                                                          \
               : "memory")
#define G2_STEP(set, bs, MML)                                                                 \
  do {                                                                                        \
    WAIT_L(0);                                                                                \
    SCHED;                                                                                    \
    _Pragma("unroll") for (int i = 0; i < 4; ++i) G2_LDB(set, i);                             \
    SCHED;                                                                                    \
    _Pragma("unroll") for (int m = 0; m < 8; ++m) {                                           \
      MML(set, bs, m, 35);                                                                    \
      if (m == 3) { WAIT_V(28); G2_RDB((bs) ^ 1, ((set) + 1) % 3); }                          \
      SCHED;                                                                                  \
    }                                                                                         \
    G2_ADV;                                                                                   \
  } while (0)
#define G2_TAIL(set, bs, NEW, NEXT)                                                           \
  do {                                                                                        \
    WAIT_L(0);                                                                                \
    SCHED;                                                                                    \
    _Pragma("unroll") for (int m = 0; m < 8; ++m) {                                           \
      G2_MM(set, bs, m, (NEW) + 7 - m);                                                       \
      if (NEXT && m == 3) {                                                                   \
        asm volatile("s_waitcnt vmcnt(%0)" ::"i"((NEW) - 4) : "memory");                      \
        G2_RDB((bs) ^ 1, ((set) + 1) % 3);                                                    \
      }                                                                                       \
      SCHED;                                                                                  \
    }                                                                                         \
  } while (0)
  G2_STEP(0, 0, G2_MML0);
  for (int t = 0; t < 4; ++t) {
    G2_STEP(1, 1, G2_MML);
    G2_STEP(2, 0, G2_MML);
    G2_STEP(0, 1, G2_MML);
    G2_STEP(1, 0, G2_MML);
    G2_STEP(2, 1, G2_MML);
    G2_STEP(0, 0, G2_MML);
  }
  G2_TAIL(1, 1, 24, true);
  G2_TAIL(2, 0, 12, true);
  G2_TAIL(0, 1, 0, false);
  asm volatile("s_nop 7\n\ts_nop 7\n\ts_nop 7" ::: "memory");
#pragma unroll
  for (int m = 0; m < 8; m += 2)
    asm volatile("" : "+a"(acc[0][m]), "+a"(acc[1][m]), "+a"(acc[2][m]), "+a"(acc[3][m]), "+a"(acc[0][m + 1]),
                      "+a"(acc[1][m + 1]), "+a"(acc[2][m + 1]), "+a"(acc[3][m + 1]) : : "memory");
  f32x4 gbv[4];
#pragma unroll
  for (int n = 0; n < 4; ++n) gbv[n] = *(const f32x4*)(modf + d0 + 16 * n + 4 * g);
  SCHED;

  TS(4, 1);
  BAR;
  f32x4* rb = (f32x4*)smem + lane;
#pragma unroll
  for (int j = 1; j < 4; ++j) {
    f32x4* dst = rb + (((w + j) & 3) * 3 + (j - 1)) * 512;
#pragma unroll
    for (int mm = 0; mm < 2; ++mm)
#pragma unroll
      for (int n = 0; n < 4; ++n) dst[(mm * 4 + n) * 64] = acc[n][2 * j + mm];
  }
  SYNC_L;
  f32x4 fin[2][4];
#pragma unroll
  for (int mm = 0; mm < 2; ++mm)
#pragma unroll
    for (int n = 0; n < 4; ++n) {
      fin[mm][n] = acc[n][mm];
#pragma unroll
      for (int j = 0; j < 3; ++j) fin[mm][n] += rb[((w * 3 + j) * 8 + mm * 4 + n) * 64];
    }
  TS(4, 2);
  {
    const char* xb = smem + G2_XOFF + w * 4096 + (4 * g + q) * 64 + p * 8;
    char* ob = (char*)out + (size_t)(s0 + 32 * w + 16 * (g & 1) + (lane & 15)) * (D_ * 2) + (d0 + 8 * (g >> 1)) * 2;
#pragma unroll
    for (int n = 0; n < 4; ++n) {
      u32 o[2][2];
#pragma unroll
      for (int mm = 0; mm < 2; ++mm) {
        s16x4 xv = __builtin_amdgcn_ds_read_tr16_b64_v4i16((lds_s16x4p)(xb + n * 1024 + mm * 32));
        u32x2 xu = __builtin_bit_cast(u32x2, xv);
        o[mm][0] = pk2(fmaf(gbv[n][0], fin[mm][n][0], bflo(xu[0])), fmaf(gbv[n][1], fin[mm][n][1], bfhi(xu[0])));
        o[mm][1] = pk2(fmaf(gbv[n][2], fin[mm][n][2], bflo(xu[1])), fmaf(gbv[n][3], fin[mm][n][3], bfhi(xu[1])));
      }
      auto e0 = __builtin_amdgcn_permlane16_swap(o[0][0], o[1][0], false, false);
      auto e1 = __builtin_amdgcn_permlane16_swap(o[0][1], o[1][1], false, false);
      u32x4 ov = {e0[0], e1[0], e0[1], e1[1]};
      *(u32x4*)(ob + n * 32) = ov;
    }
  }
  TS(4, 3);
}

extern "C" void kernel_launch(void* const* d_in, const int* in_sizes, int n_in, void* d_out, int out_size,
                              void* d_ws, size_t ws_size, hipStream_t stream) {
  const u16* x = (const u16*)d_in[0];
  const u16* k = (const u16*)d_in[1];
  const u16* v = (const u16*)d_in[2];
  const u16* shk = (const u16*)d_in[3];
  const u16* ssmk = (const u16*)d_in[4];
  const u16* ssmD = (const u16*)d_in[5];
  const u16* w1 = (const u16*)d_in[7];
  const u16* w3 = (const u16*)d_in[8];
  const u16* w2 = (const u16*)d_in[9];
  const u16* c = (const u16*)d_in[10];
  const u16* wmod = (const u16*)d_in[11];
  char* ws = (char*)d_ws;
  u16* kvT = (u16*)(ws + WS_KVT);
  u16* xT = (u16*)(ws + WS_XT);
  u16* x1T = (u16*)(ws + WS_X1T);
  u16* apT = (u16*)(ws + WS_APT);
  u16* hh = (u16*)(ws + WS_HHT);
  float* modp = (float*)(ws + WS_MODP);
  f32x2* st1 = (f32x2*)(ws + WS_ST1);
  float* modf = (float*)(ws + WS_MOD);
  f32x2* st2 = (f32x2*)(ws + WS_ST2);
  float* c12 = (float*)(ws + WS_C12);

  (void)hipFuncSetAttribute((const void*)k1_conv, hipFuncAttributeMaxDynamicSharedMemorySize, K1_LDS);
  (void)hipFuncSetAttribute((const void*)g1_gemm, hipFuncAttributeMaxDynamicSharedMemorySize, G1_LDS);
  (void)hipFuncSetAttribute((const void*)g2_gemm, hipFuncAttributeMaxDynamicSharedMemorySize, G2_LDS);

  k0_prep<<<K0_TRB + K0_MODB + K0_STB, 256, 0, stream>>>(x, k, v, shk, c, wmod, kvT, xT, st1, modp, st2);
  k1_conv<<<256, 768, K1_LDS, stream>>>(ssmk, ssmD, kvT, xT, st1, modp, w1, w3, x1T, apT, modf, c12);
  g1_gemm<<<256, 512, G1_LDS, stream>>>(apT, w1, w3, x1T, st1, (unsigned long long*)st2, c12, w2, hh);
  g2_gemm<<<256, 256, G2_LDS, stream>>>(hh, w2, x1T, modf, (u16*)d_out);
}
```

```cpp
#include <hip/hip_runtime.h>
#include <hip/hip_bf16.h>
#include <stdint.h>

typedef unsigned short u16;
typedef unsigned int u32;
typedef __attribute__((ext_vector_type(8))) __bf16 bf16x8;
typedef __attribute__((ext_vector_type(2))) __bf16 bf16x2;
typedef __attribute__((ext_vector_type(4))) short s16x4;
typedef __attribute__((ext_vector_type(8))) short s16x8;
typedef __attribute__((ext_vector_type(4))) float f32x4;
typedef __attribute__((ext_vector_type(2))) float f32x2;
typedef __attribute__((ext_vector_type(4))) u32 u32x4;
typedef __attribute__((ext_vector_type(2))) u32 u32x2;

#define SLAB_EARLY 0
#define K1_REMAP 1
#define S_ 2048
#define D_ 1024
#define F_ 3584

#define WS_KVT (0u)
#define WS_XT (4u << 20)
#define WS_X1T (8u << 20)
#define WS_APT (12u << 20)
#define WS_HHT (16u << 20)
#define WS_MODP (32u << 20)
#define WS_ST1 (33u << 20)
#define WS_MOD ((33u << 20) + (64u << 10))
#define WS_ST2 ((33u << 20) + (128u << 10))
#define WS_C12 ((33u << 20) + (192u << 10))

__device__ __forceinline__ float bflo(u32 u) { return __uint_as_float(u << 16); }
__device__ __forceinline__ float bfhi(u32 u) { return __uint_as_float(u & 0xffff0000u); }
__device__ __forceinline__ float bf1(u16 h) { return __uint_as_float(((u32)h) << 16); }
__device__ __forceinline__ u32 pk2(float a, float b) {
  bf16x2 r = {(__bf16)a, (__bf16)b};
  return __builtin_bit_cast(u32, r);
}
__device__ __forceinline__ float rbf(float a) {
  return __uint_as_float(((u32)__builtin_bit_cast(u16, (__bf16)a)) << 16);
}
__device__ __forceinline__ float silu_f(float g) { return g * __builtin_amdgcn_rcpf(1.0f + __expf(-g)); }

#define GLDS16(gptr, ldsptr)                                                        \
  __builtin_amdgcn_global_load_lds((const __attribute__((address_space(1))) u32*)(gptr), \
                                   (__attribute__((address_space(3))) u32*)(ldsptr), 16, 0, 0)
#define WAIT_V(n) asm volatile("s_waitcnt vmcnt(" #n ")" ::: "memory")
#define WAIT_L(n) asm volatile("s_waitcnt lgkmcnt(" #n ")" ::: "memory")
#define BAR __builtin_amdgcn_s_barrier()
#define SCHED __builtin_amdgcn_sched_barrier(0)
#define STP0 ""
#define STP1 "sc1"
#define STPG "sc1"
#define ST16(ptr, val, POL) asm volatile("global_store_dwordx4 %0, %1, off " POL "\n\ts_nop 1" ::"v"(ptr), "v"(val) : "memory")
#define ST8(ptr, val, POL) asm volatile("global_store_dwordx2 %0, %1, off " POL ::"v"(ptr), "v"(val) : "memory")

#define TS(k, i)
#define TSC(k, i)
#define TSW(k, i, wv)

__device__ __forceinline__ float wave_sum(float v) {
#pragma unroll
  for (int o = 32; o > 0; o >>= 1) v += __shfl_xor(v, o, 64);
  return v;
}

#define K0_MODB 384
#define K0_STB 256
#define K0_TRB 256
__global__ __launch_bounds__(256) void k0_prep(const u16* __restrict__ x, const u16* __restrict__ kk,
                                               const u16* __restrict__ vv, const u16* __restrict__ shk,
                                               const u16* __restrict__ cvec, const u16* __restrict__ wmod,
                                               u16* __restrict__ kvT, u16* __restrict__ xT,
                                               f32x2* __restrict__ st1, float* __restrict__ modp,
                                               f32x2* __restrict__ st2) {
  __shared__ float red[8 * 256];
  const int tid = threadIdx.x;
  int b = blockIdx.x;
  TS(0, 0);
  if (b < K0_TRB) {
    const int dt = b & 15, stile = b >> 4;
    const int lane = tid & 63, w = tid >> 6;
    const int c8 = lane & 7, sq = lane >> 3;
    const int s0 = stile * 128 + w * 32 + sq * 4;
    const int d0 = dt * 64 + c8 * 8;
    u32x4 kr[7], vr[4], xr[4], sw[4];
#pragma unroll
    for (int i = 0; i < 7; ++i) {
      int s = s0 - 3 + i;
      int sc = s < 0 ? 0 : s;
      kr[i] = *(const u32x4*)(kk + (size_t)sc * D_ + d0);
    }
#pragma unroll
    for (int i = 0; i < 4; ++i) vr[i] = *(const u32x4*)(vv + (size_t)(s0 + i) * D_ + d0);
#pragma unroll
    for (int i = 0; i < 4; ++i) xr[i] = *(const u32x4*)(x + (size_t)(s0 + i) * D_ + d0);
#pragma unroll
    for (int i = 0; i < 4; ++i) sw[i] = *(const u32x4*)(shk + (size_t)d0 * 4 + i * 8);
    if (s0 == 0) {
#pragma unroll
      for (int i = 0; i < 3; ++i) kr[i] = (u32x4){0, 0, 0, 0};
    }
#pragma unroll
    for (int j = 0; j < 8; ++j) {
      u32 wa = sw[j >> 1][(j & 1) * 2], wb = sw[j >> 1][(j & 1) * 2 + 1];
      float w0 = bflo(wa), w1 = bfhi(wa), w2 = bflo(wb), w3 = bfhi(wb);
      float kf[7];
#pragma unroll
      for (int i = 0; i < 7; ++i) kf[i] = (j & 1) ? bfhi(kr[i][j >> 1]) : bflo(kr[i][j >> 1]);
      float o[4];
#pragma unroll
      for (int i = 0; i < 4; ++i) {
        float ks = kf[i] * w3;
        ks = fmaf(kf[i + 1], w2, ks);
        ks = fmaf(kf[i + 2], w1, ks);
        ks = fmaf(kf[i + 3], w0, ks);
        float vf = (j & 1) ? bfhi(vr[i][j >> 1]) : bflo(vr[i][j >> 1]);
        o[i] = ks * vf;
      }
      u32x2 ov = {pk2(o[0], o[1]), pk2(o[2], o[3])};
      ST8(kvT + (size_t)(d0 + j) * S_ + s0, ov, STP0);
      const u32 sel = (j & 1) ? 0x07060302u : 0x05040100u;
      u32x2 xv = {__builtin_amdgcn_perm(xr[1][j >> 1], xr[0][j >> 1], sel),
                  __builtin_amdgcn_perm(xr[3][j >> 1], xr[2][j >> 1], sel)};
      ST8(xT + (size_t)(d0 + j) * S_ + s0, xv, STP0);
    }
    TS(0, 3);
    return;
  }
  b -= K0_TRB;
  if (b < K0_MODB) {
    const int ks = b / 24, strip = b % 24;
    const int cl = tid & 31, rg = tid >> 5;
    u32x4 wv[8];
    u16 cv[8];
#pragma unroll
    for (int i = 0; i < 8; ++i) {
      int row = ks * 64 + rg + 8 * i;
      wv[i] = *(const u32x4*)(wmod + (size_t)row * (6 * D_) + strip * 256 + cl * 8);
      cv[i] = cvec[row];
    }
    float acc[8];
#pragma unroll
    for (int j = 0; j < 8; ++j) acc[j] = 0.f;
#pragma unroll
    for (int i = 0; i < 8; ++i) {
      float cf = bf1(cv[i]);
      float sc = rbf(cf / (1.0f + __expf(-cf)));
#pragma unroll
      for (int j = 0; j < 4; ++j) {
        acc[2 * j] = fmaf(sc, bflo(wv[i][j]), acc[2 * j]);
        acc[2 * j + 1] = fmaf(sc, bfhi(wv[i][j]), acc[2 * j + 1]);
      }
    }
    *(f32x4*)&red[rg * 256 + cl * 8] = (f32x4){acc[0], acc[1], acc[2], acc[3]};
    *(f32x4*)&red[rg * 256 + cl * 8 + 4] = (f32x4){acc[4], acc[5], acc[6], acc[7]};
    __syncthreads();
    float s = 0.f;
#pragma unroll
    for (int i = 0; i < 8; ++i) s += red[i * 256 + tid];
    modp[ks * (6 * D_) + strip * 256 + tid] = s;
    TS(0, 3);
    return;
  }
  b -= K0_MODB;
  {
    const int lane = tid & 63, w = tid >> 6;
    const int r0 = b * 8 + w * 2;
    u32x4 a[2][2];
#pragma unroll
    for (int r = 0; r < 2; ++r)
#pragma unroll
      for (int h = 0; h < 2; ++h) a[r][h] = *(const u32x4*)(x + (size_t)(r0 + r) * D_ + h * 512 + lane * 8);
#pragma unroll
    for (int r = 0; r < 2; ++r) {
      float s = 0.f;
#pragma unroll
      for (int h = 0; h < 2; ++h)
#pragma unroll
        for (int j = 0; j < 4; ++j) s += bflo(a[r][h][j]) + bfhi(a[r][h][j]);
      s = wave_sum(s);
      float m = s * (1.0f / D_);
      float q = 0.f;
#pragma unroll
      for (int h = 0; h < 2; ++h)
#pragma unroll
        for (int j = 0; j < 4; ++j) {
          float d0 = bflo(a[r][h][j]) - m, d1 = bfhi(a[r][h][j]) - m;
          q = fmaf(d0, d0, q);
          q = fmaf(d1, d1, q);
        }
      q = wave_sum(q);
      float rs = rsqrtf(q * (1.0f / D_) + 1e-5f);
      if (lane == 0) {
        st1[r0 + r] = (f32x2){m, rs};
        st2[r0 + r] = (f32x2){0.f, 0.f};
      }
    }
    TS(0, 3);
  }
}

#define K1_KOFF 0
#define K1_UOFF 4224
#define K1_XOFF 8576
#define K1_YOFF 12672
#define K1_COFF 20864
#define K1_WSZ 21408
#define K1_STOFF (4 * K1_WSZ)
#define K1_SLOFF (K1_STOFF + 16384)
#define K1_MVOFF (K1_SLOFF + 57344)
#define K1_LDS (K1_MVOFF + 512)

typedef __attribute__((address_space(3))) s16x4* lds_s16x4p;
__device__ __forceinline__ bf16x8 tr_frag(const char* p0, const char* p1) {
  s16x4 a = __builtin_amdgcn_ds_read_tr16_b64_v4i16((lds_s16x4p)p0);
  s16x4 b = __builtin_amdgcn_ds_read_tr16_b64_v4i16((lds_s16x4p)p1);
  s16x8 c = __builtin_shufflevector(a, b, 0, 1, 2, 3, 4, 5, 6, 7);
  return __builtin_bit_cast(bf16x8, c);
}
template <int CTRL>
__device__ __forceinline__ float dppz(float v) {
  return __builtin_bit_cast(float, __builtin_amdgcn_update_dpp(0, __builtin_bit_cast(int, v), CTRL, 0xf, 0xf, true));
}
#define TRRD(dst, addr, off) \
  asm volatile("ds_read_b64_tr_b16 %0, %1 offset:%2" : "=v"(dst) : "v"(addr), "i"(off))
#define SYNC_ALL asm volatile("s_waitcnt vmcnt(0) lgkmcnt(0)\n\ts_barrier" ::: "memory")
#define SYNC_L asm volatile("s_waitcnt lgkmcnt(0)\n\ts_barrier" ::: "memory")

__global__ __launch_bounds__(512, 2) void k1_conv(const u16* __restrict__ ssmk, const u16* __restrict__ ssmD,
                                                  const u16* __restrict__ kvT, const u16* __restrict__ xT,
                                                  const f32x2* __restrict__ st1, const float* __restrict__ modp,
                                                  const u16* __restrict__ w1, const u16* __restrict__ w3,
                                                  u16* __restrict__ x1T, u16* __restrict__ apT,
                                                  float* __restrict__ modf, float* __restrict__ c12) {
  extern __shared__ __attribute__((aligned(16))) char smem[];
  const int tid = threadIdx.x, lane = tid & 63;
  const int w = __builtin_amdgcn_readfirstlane(tid >> 6);
  TS(1, 0);
  const int c = w & 3, h = w >> 2;
  const int ch = ((blockIdx.x & 7) + 8 * (blockIdx.x >> 7)) * 64 + ((blockIdx.x >> 3) & 15) * 4 + c;
  char* wb = smem + c * K1_WSZ;
  {
    const u16* src = (h ? kvT : ssmk) + (size_t)ch * S_ + lane * 8;
    char* dst = wb + (h ? K1_UOFF + 256 : K1_KOFF + 128);
#pragma unroll
    for (int i = 0; i < 4; ++i) GLDS16(src + i * 512, dst + i * 1024);
    SCHED;
#pragma unroll
    for (int i = 0; i < 2; ++i) {
      GLDS16(xT + (size_t)ch * S_ + (2 * h + i) * 512 + lane * 8, wb + K1_XOFF + (2 * h + i) * 1024);
      GLDS16((const char*)st1 + (w * 2 + i) * 1024 + lane * 16, smem + K1_STOFF + (w * 2 + i) * 1024);
    }
  }
  SCHED;
  float mv0, mv1;
  u32 ddu;
  {
    const u32 o0 = ((lane & 15) * (6 * D_) + (lane >> 4) * D_) * 4;
    const u32 o1 = ((lane & 15) * (6 * D_) + (4 + ((lane >> 4) & 1)) * D_) * 4;
    asm volatile("global_load_dword %0, %1, %2" : "=v"(mv0) : "v"(o0), "s"(modp + ch) : "memory");
    asm volatile("global_load_dword %0, %1, %2" : "=v"(mv1) : "v"(o1), "s"(modp + ch) : "memory");
    asm volatile("global_load_ushort %0, %1, %2" : "=v"(ddu) : "v"(0u), "s"(ssmD + ch) : "memory");
  }
  const int nt = (blockIdx.x & 7) * 4 + ((blockIdx.x >> 3) & 3), ks = blockIdx.x >> 5;
  f32x4 ms0, ms1;
  {
    const u32 o = ((lane & 15) * (6 * D_) + (lane >> 4) * 4) * 4;
    const float* mb = modp + 3 * D_ + ks * 128 + w * 16;
    asm volatile("global_load_dwordx4 %0, %1, %2" : "=v"(ms0) : "v"(o), "s"(mb + D_) : "memory");
    asm volatile("global_load_dwordx4 %0, %1, %2" : "=v"(ms1) : "v"(o), "s"(mb) : "memory");
  }
  SCHED;
#define K1_SLAB(i)                                                                                   \
  do {                                                                                               \
    const int u_ = (i) * 64 + lane;                                                                  \
    const int um_ = u_ >= 224 ? u_ - 224 : u_;                                                       \
    const u16* wp_ = (u_ >= 224 ? w3 : w1) + (size_t)(ks * 128 + w * 16) * F_ + nt * 112;            \
    GLDS16((const char*)wp_ + (u32)((um_ / 14) * (F_ * 2) + (um_ % 14) * 16),                        \
           smem + K1_SLOFF + w * 7168 + (i) * 1024);                                                 \
  } while (0)
  const u32 lwb = (u32)(uintptr_t)(__attribute__((address_space(3))) char*)wb;
  {
    const u32 za = lwb + (h ? K1_UOFF + (lane & 15) * 16 : ((lane & 15) < 8 ? K1_KOFF + (lane & 7) * 16 : K1_COFF));
    const u32x4 z4 = {0, 0, 0, 0};
    if (lane < 16) asm volatile("ds_write_b128 %0, %1" ::"v"(za), "v"(z4) : "memory");
  }
  const int G = lane >> 4, q = (lane >> 2) & 3, p = lane & 3;
  const u32 abase = lwb + K1_KOFF + 128 - 96 + 32 * p + 8 * (8 * G + q);
  const u32 bbase = lwb + K1_UOFF + 256 + 96 - 32 + 8 * p - 8 * (8 * G + q) + 128 * h;
  const u32 yb = lwb + K1_YOFF + 4 * (64 * h + lane);
  const u32 cy = lwb + K1_COFF + 16 * (h + 1);
  const u32 lds0 = (u32)(uintptr_t)(__attribute__((address_space(3))) char*)smem;
  const u32 mva = lds0 + K1_MVOFF + (w >> 1) * 128 + G * 16 + (w & 1) * 8;
  asm volatile("s_waitcnt vmcnt(9) lgkmcnt(0)\n\ts_barrier" ::: "memory");
  TS(1, 1);
  TSC(5, 2);

  u32x2 Af[16][2], Bf[16][2];
#define K1_RD(a)                               \
  do {                                         \
    TRRD(Af[a][0], abase, 256 * (a));          \
    TRRD(Af[a][1], abase, 256 * (a) + 32);     \
    TRRD(Bf[a][0], bbase, 256 * (a) + 32);     \
    TRRD(Bf[a][1], bbase, 256 * (a));          \
  } while (0)
  K1_RD(0); K1_RD(1); K1_RD(2);
  f32x4 zp = {0, 0, 0, 0};
  float sa, ca1, ga, cb1, dd;
#pragma unroll
  for (int a = 0; a < 17; ++a) {
    if (a < 16) {
      const int n = a < 2 ? 8 : a == 2 ? 10 : a == 3 ? 12 : a < 14 ? 14 : a == 14 ? 10 : 6;
      asm volatile("s_waitcnt lgkmcnt(%0)" ::"i"(n) : "memory");
    }
    SCHED;
    if (a + 3 < 16) K1_RD(a + 3);
    if (a >= 1 && a < 8) { K1_SLAB(a - 1); SCHED; }
    f32x4 z = {0, 0, 0, 0};
    if (a < 16) {
#pragma unroll
      for (int i = 0; i <= a; ++i) {
        u32x4 a4 = {Af[i][0][0], Af[i][0][1], Af[i][1][0], Af[i][1][1]};
        u32x4 b4 = {Bf[a - i][0][0], Bf[a - i][0][1], Bf[a - i][1][0], Bf[a - i][1][1]};
        z = __builtin_amdgcn_mfma_f32_16x16x32_bf16(__builtin_bit_cast(bf16x8, a4), __builtin_bit_cast(bf16x8, b4),
                                                    z, 0, 0, 0);
      }
    }
    if (a > 0) {
      float w2 = dppz<0x138>(zp[3]) + zp[2];
      float w1 = dppz<0x138>(w2) + zp[1];
      float y = dppz<0x138>(w1) + zp[0];
      f32x4 cv = {w1, w2, zp[3], 0.f};
      asm volatile("ds_write_b32 %0, %1 offset:%2" ::"v"(yb), "v"(y), "i"(512 * (a - 1)) : "memory");
      asm volatile("s_mov_b64 exec, %3\n\tds_write_b128 %0, %1 offset:%2\n\ts_mov_b64 exec, -1"
                   ::"v"(cy), "v"(cv), "i"(32 * (a - 1)), "s"(0x8000000000000000ull) : "memory");
    }
    if (a == 8) TSC(5, 5);
    if (a == 12) TSC(5, 6);
    if (a == 12) {
      asm volatile("s_waitcnt vmcnt(7)" : "+v"(mv0), "+v"(mv1), "+v"(ddu), "+v"(ms0), "+v"(ms1) : : "memory");
#define RSUM16(v) do { v += dppz<0xB1>(v); v += dppz<0x4E>(v); v += dppz<0x141>(v); v += dppz<0x140>(v); } while (0)
      RSUM16(mv0);
      RSUM16(mv1);
      mv0 = rbf(mv0);
      mv1 = rbf(mv1);
      const float m0p = rbf(1.0f + mv0), m1p = rbf(1.0f + mv1);
#define RDL(v, l) __builtin_bit_cast(float, __builtin_amdgcn_readlane(__builtin_bit_cast(int, v), l))
      sa = RDL(mv0, 0); ca1 = RDL(m0p, 16); ga = RDL(mv0, 32); cb1 = RDL(m1p, 0);
      dd = bflo(ddu);
      if (h == 0 && lane == 16) modf[ch] = mv1;
    }
    if (a == 13 || a == 14) {
      f32x4 m = a == 13 ? ms0 : ms1;
#pragma unroll
      for (int r = 0; r < 4; ++r) {
        float t = m[r];
        RSUM16(t);
        if (a == 13) t = 1.0f + rbf(t);
        m[r] = t;
      }
      u32x2 mv = {pk2(m[0], m[1]), pk2(m[2], m[3])};
      asm volatile("s_mov_b64 exec, %3\n\tds_write_b64 %0, %1 offset:%2\n\ts_mov_b64 exec, -1"
                   ::"v"(mva), "v"(mv), "i"(64 * (a - 13)), "s"(0x0001000100010001ull) : "memory");
    }
    zp = z;
    SCHED;
  }
  TSC(5, 3);
  SYNC_L;
  TS(1, 2);
  TSC(5, 4);
#pragma unroll
  for (int it = 0; it < 2; ++it) {
    const int so = h * 1024 + it * 512 + lane * 8;
    f32x4 y0 = *(const f32x4*)(wb + K1_YOFF + so * 4);
    f32x4 y1 = *(const f32x4*)(wb + K1_YOFF + so * 4 + 16);
    f32x4 cv = *(const f32x4*)(wb + K1_COFF + ((lane & 7) ? 0 : (so >> 6) * 16));
    u32x4 uv = *(const u32x4*)(wb + K1_UOFF + 256 + so * 2);
    u32x4 xv = *(const u32x4*)(wb + K1_XOFF + so * 2);
    y0 += cv;
    u32x4 o1, o2;
#pragma unroll
    for (int j = 0; j < 4; ++j) {
      f32x4 st = *(const f32x4*)(smem + K1_STOFF + (so + 2 * j) * 8);
      float r[2], ap[2];
#pragma unroll
      for (int e = 0; e < 2; ++e) {
        float xf = e ? bfhi(xv[j]) : bflo(xv[j]);
        float yf = j < 2 ? y0[2 * j + e] : y1[2 * j - 4 + e];
        float uf = e ? bfhi(uv[j]) : bflo(uv[j]);
        float m1 = st[2 * e], r1 = st[2 * e + 1];
        float h1 = fmaf((xf - m1) * r1, ca1, sa);
        float yy = fmaf(uf, dd, yf);
        r[e] = fmaf(ga, h1 * yy, xf);
      }
      o1[j] = pk2(r[0], r[1]);
      ap[0] = (bflo(o1[j]) - st[0]) * cb1;
      ap[1] = (bfhi(o1[j]) - st[2]) * cb1;
      o2[j] = pk2(ap[0], ap[1]);
    }
    ST16(x1T + (size_t)ch * S_ + so, o1, STP1);
    ST16(apT + (size_t)ch * S_ + so, o2, STP1);
  }
  TS(1, 3);
  SCHED;
  asm volatile("s_waitcnt vmcnt(4)\n\ts_barrier" ::: "memory");
  {
    const int f1 = w < 6 ? w + 8 : w;
    const u32 ra = lds0 + K1_SLOFF + (4 * G + q) * 224 + p * 8;
    const u32 a0 = ra + (w >= 7 ? 3584 + (w - 7) * 32 : w * 32);
    const u32 a1 = ra + (f1 >= 7 ? 3584 + (f1 - 7) * 32 : f1 * 32);
    const u32 mb = lds0 + K1_MVOFF + (lane & 1) * 64 + G * 16;
    u32x2 fa[4][2], fb[4][2];
    u32x4 mf[4];
#pragma unroll
    for (int k = 0; k < 4; ++k) {
      asm volatile("ds_read_b128 %0, %1 offset:%2" : "=v"(mf[k]) : "v"(mb), "i"(128 * k));
      TRRD(fa[k][0], a0, 14336 * k);
      TRRD(fa[k][1], a0, 14336 * k + 7168);
      TRRD(fb[k][0], a1, 14336 * k);
      TRRD(fb[k][1], a1, 14336 * k + 7168);
    }
    WAIT_L(0);
    SCHED;
    f32x4 z0 = {0, 0, 0, 0}, z1 = {0, 0, 0, 0};
#pragma unroll
    for (int k = 0; k < 4; ++k) {
      u32x4 x0 = {fa[k][0][0], fa[k][0][1], fa[k][1][0], fa[k][1][1]};
      u32x4 x1 = {fb[k][0][0], fb[k][0][1], fb[k][1][0], fb[k][1][1]};
      z0 = __builtin_amdgcn_mfma_f32_16x16x32_bf16(__builtin_bit_cast(bf16x8, x0), __builtin_bit_cast(bf16x8, mf[k]), z0, 0, 0, 0);
      z1 = __builtin_amdgcn_mfma_f32_16x16x32_bf16(__builtin_bit_cast(bf16x8, x1), __builtin_bit_cast(bf16x8, mf[k]), z1, 0, 0, 0);
    }
    if ((lane & 15) < 2) {
      float* cp = c12 + (ks * 32 + nt) * 448 + (lane & 1) * 224 + 4 * G;
      *(f32x4*)(cp + 16 * w) = z0;
      if (w < 6) *(f32x4*)(cp + 16 * f1) = z1;
    }
  }
  TS(1, 4);
}

#define ABL_BAR "s_barrier"
#define ABL_RD 22
#define ABL_G 1
#define GP0 22
#define GPS 1
#define G1_SLOT 32768
#define G1_R 5
#define G1_BOFF 16384
#define G1_LDS (G1_R * G1_SLOT)

typedef __attribute__((address_space(1))) unsigned long long gu64;

__device__ __forceinline__ void wf_merge(float& m, float& q, float ma, float qa, float mb, float qb, float hn) {
  float d = mb - ma;
  m = 0.5f * (ma + mb);
  q = fmaf(d * hn, d, qa + qb);
}
__device__ __forceinline__ void wf_swap32(float& m, float& q, float ma, float qa, float mb, float qb, float hn) {
  auto a = __builtin_amdgcn_permlane32_swap(__float_as_uint(ma), __float_as_uint(mb), false, false);
  auto c = __builtin_amdgcn_permlane32_swap(__float_as_uint(qa), __float_as_uint(qb), false, false);
  wf_merge(m, q, __uint_as_float(a[0]), __uint_as_float(c[0]), __uint_as_float(a[1]), __uint_as_float(c[1]), hn);
}
__device__ __forceinline__ void wf_swap16(float& m, float& q, float ma, float qa, float mb, float qb, float hn) {
  auto a = __builtin_amdgcn_permlane16_swap(__float_as_uint(ma), __float_as_uint(mb), false, false);
  auto c = __builtin_amdgcn_permlane16_swap(__float_as_uint(qa), __float_as_uint(qb), false, false);
  wf_merge(m, q, __uint_as_float(a[0]), __uint_as_float(c[0]), __uint_as_float(a[1]), __uint_as_float(c[1]), hn);
}
template <int CTRL>
__device__ __forceinline__ void wf_dpp(float& m, float& q, float hn) {
  float mo = dppz<CTRL>(m), qo = dppz<CTRL>(q);
  wf_merge(m, q, m, q, mo, qo, hn);
}

__global__ __launch_bounds__(512, 2) void g1_gemm(const u16* __restrict__ apT, const u16* __restrict__ w1,
                                                  const u16* __restrict__ w3, const u16* __restrict__ x1T,
                                                  const f32x2* __restrict__ st1, unsigned long long* st2,
                                                  const float* __restrict__ c12, const u16* __restrict__ w2,
                                                  u16* __restrict__ hh) {
  extern __shared__ __attribute__((aligned(16))) char smem[];
  const int tid = threadIdx.x, lane = tid & 63;
  const int w = __builtin_amdgcn_readfirstlane(tid >> 6);
  const int grp = w >> 2;
  const int wm = w & 3, wn = grp;
  const int b = blockIdx.x;
  const int xcd = b & 7, idx = b >> 3;
  const int mt = idx & 7, nt = xcd * 4 + (idx >> 3);
  const int s0 = mt * 256, f0 = nt * 112;
  TS(3, 0);

  const u32 lds0 = (u32)(uintptr_t)(__attribute__((address_space(3))) char*)smem;
  u32 voa[2], vob;
#pragma unroll
  for (int j = 0; j < 2; ++j) {
    int row = 4 * w + 2 * j + (lane >> 5);
    int c = (lane & 31) ^ ((row & 7) << 1);
    voa[j] = row * (S_ * 2) + c * 16 - j * 1024;
  }
  {
    int li = lane - 8 * (w & 1);
    li = li < 0 ? 0 : (li > 55 ? 55 : li);
    vob = (4 * w + li / 14) * (F_ * 2) + (li % 14) * 16;
  }
  const unsigned long long bmask = (w & 1) ? 0xFFFFFFFFFFFFFF00ull : 0x00FFFFFFFFFFFFFFull;
  const char* sa_ = (const char*)apT + s0 * 2;
  const char* sb1 = (const char*)w1 + f0 * 2 - 2048;
  const char* sb3 = (const char*)w3 + f0 * 2 - 3072;
  u32 wdst = lds0 + w * 4096;
#define G1_GA(j)                                                                                   \
  asm volatile("s_mov_b32 m0, %0\n\ts_nop 0\n\tglobal_load_lds_dwordx4 %1, %2 offset:%3"           \
               ::"s"(wdst), "v"(voa[j]), "s"(sa_), "i"((j) * 1024) : "memory")
#define GBX "s_mov_b32 m0, %0\n\ts_mov_b64 exec, %4\n\tglobal_load_lds_dwordx4 %1, %2 offset:%3\n\ts_mov_b64 exec, -1"
#define G1_GB(sb, off)                                                                             \
  asm volatile(GBX \
               ::"s"(wdst), "v"(vob), "s"(sb), "i"(off), "s"(bmask) : "memory")
#define G1_ADV                                                                                     \
  do {                                                                                             \
    sa_ += 32 * S_ * 2; sb1 += 32 * F_ * 2; sb3 += 32 * F_ * 2;                                    \
    wdst += G1_SLOT; if (wdst >= lds0 + G1_R * G1_SLOT) wdst -= G1_R * G1_SLOT;                    \
  } while (0)
#pragma unroll
  for (int i = 0; i < G1_R; ++i) {
    G1_GB(sb1, 2048); G1_GB(sb3, 3072); G1_GA(0); G1_GA(1);
    G1_ADV;
  }
  SCHED;

  const int r0 = (xcd * 32 + idx) * 8;
  u32x4 xa, xb;
  float m1v;
  {
    const u32 xo = tid * (S_ * 2);
    const char* xbase0 = (const char*)x1T + r0 * 2;
    const char* xbase1 = xbase0 + 512 * (S_ * 2);
    asm volatile("global_load_dwordx4 %0, %1, %2" : "=v"(xa) : "v"(xo), "s"(xbase0) : "memory");
    asm volatile("global_load_dwordx4 %0, %1, %2" : "=v"(xb) : "v"(xo), "s"(xbase1) : "memory");
    const u32 mo = (lane & 7) * 8;
    asm volatile("global_load_dword %0, %1, %2" : "=v"(m1v) : "v"(mo), "s"(st1 + r0) : "memory");
  }
  SCHED;

  u32 ard[4], brd[2];
  {
    const int g = lane >> 4, q = (lane >> 2) & 3, p = lane & 3;
    const int row = 4 * g + q;
#pragma unroll
    for (int m = 0; m < 4; ++m) {
      int c = (8 * wm + 2 * m + (p >> 1)) ^ ((row & 7) << 1);
      ard[m] = lds0 + g * 4096 + q * 512 + c * 16 + 8 * (p & 1);
    }
    const u32 bb = lds0 + g * 4096 + 2048 + (g & 1) * 128 + q * 224 + 7 * wn * 16 + 8 * (p & 1);
    brd[0] = bb + (p >> 1) * 16;
    brd[1] = bb + (p >> 1) * 1024 + 96;
  }

  f32x4 acc[7][4];
  u32x2 fa[2][4][2], fb[2][7][2];
  u32 rso = 0;
  u32 ra[4], rb[2];
#define G1_RDX(TR, st, i)                                                                            \
  do {                                                                                             \
    if ((i) < 14) {                                                                                \
      const int n_ = (i) >> 1;                                                                     \
      TR(fb[st][n_][(i) & 1], rb[n_ < 6 ? 0 : 1],                                                \
           (n_ < 6 ? (n_ & 1) * 1024 + (n_ >> 1) * 32 : 0) + ((i) & 1) * 16384);                   \
    } else {                                                                                       \
      TR(fa[st][((i) - 14) >> 1][(i) & 1], ra[((i) - 14) >> 1], ((i) & 1) * 16384);              \
    }                                                                                              \
  } while (0)
#define G1_RD(st, i) G1_RDX(TRRD, st, i)
#define TRRD2(dst, addr, off) \
  asm volatile("ds_read_b64_tr_b16 %0, %1 offset:%2" : "+v"(dst) : "v"(addr), "i"(off))
#define G1_RADDR                                                                                   \
  do {                                                                                             \
    _Pragma("unroll") for (int m = 0; m < 4; ++m) ra[m] = ard[m] + rso;                            \
    rb[0] = brd[0] + rso; rb[1] = brd[1] + rso;                                                    \
    rso += G1_SLOT; if (rso == G1_R * G1_SLOT) rso = 0;                                            \
    asm volatile("" : "+s"(rso));                                                                  \
  } while (0)
#define G1_MM(Z, st, i)                                                                            \
  do {                                                                                             \
    const int m_ = (i) / 7, n_ = (i) % 7;                                                          \
    u32x4 a4 = {fa[st][m_][0][0], fa[st][m_][0][1], fa[st][m_][1][0], fa[st][m_][1][1]};           \
    u32x4 b4 = {fb[st][n_][0][0], fb[st][n_][0][1], fb[st][n_][1][0], fb[st][n_][1][1]};           \
    if (Z) asm volatile("v_mfma_f32_16x16x32_bf16 %0, %1, %2, 0" : "=&v"(acc[n_][m_]) : "v"(b4), "v"(a4)); \
    else asm volatile("v_mfma_f32_16x16x32_bf16 %0, %1, %2, %0" : "+v"(acc[n_][m_]) : "v"(b4), "v"(a4)); \
  } while (0)
#define G1_LD(TR, st, STG)                                                                         \
  do {                                                                                             \
    _Pragma("unroll") for (int i = 0; i < 22; ++i) G1_RDX(TR, 1 - (st), i);                        \
    if (STG) { G1_GB(sb1, 2048); G1_GB(sb3, 3072); G1_GA(0); G1_GA(1); G1_ADV; }                   \
    G1_RADDR;                                                                                      \
    SCHED;                                                                                         \
  } while (0)
#define G1_IT(MODE, st, VM, STG, HOOK, EXTRA)                                                      \
  do {                                                                                             \
    SCHED;                                                                                         \
    asm volatile("s_waitcnt vmcnt(" #VM ") lgkmcnt(0)\n\ts_barrier" ::: "memory");                 \
    SCHED;                                                                                         \
    EXTRA;                                                                                         \
    if (grp) G1_LD(TRRD, st, STG);                                                                 \
    SCHED;                                                                                         \
    _Pragma("unroll") for (int i = 0; i < 28; ++i) {                                               \
      G1_MM(MODE, st, i);                                                                            \
      SCHED;                                                                                       \
      HOOK;                                                                                        \
      SCHED;                                                                                       \
    }                                                                                              \
    if (!grp) G1_LD(TRRD2, st, STG);                                                               \
    SCHED;                                                                                         \
  } while (0)

  G1_RADDR;
  SCHED;
  asm volatile("s_waitcnt vmcnt(19)\n\ts_barrier" ::: "memory");
  SCHED;
#pragma unroll
  for (int i = 0; i < 22; ++i) G1_RD(0, i);
  G1_RADDR;
  SCHED;
  TS(3, 1);
  TSC(5, 0);
  unsigned long long sg[4];
  int tid2;
  gu64* sp;
#define G1_MAIN(MODE)                                                                              \
  do {                                                                                             \
    G1_IT(1, 0, 15, 1, {}, {});                                                                    \
    G1_IT(MODE, 1, 15, 1, {}, {});                                                                 \
    G1_IT(MODE, 0, 15, 1, {}, {});                                                                 \
    G1_IT(MODE, 1, 15, 1, {}, {});                                                                 \
    float smm, sqq, sm[8], sq[8];                                                                  \
    G1_IT(MODE, 0, 12, 1, {                                                                        \
      if (i == 0) { asm volatile("" : "+v"(xa), "+v"(xb), "+v"(m1v) : : "memory"); }               \
      if (i < 8) wf_merge(sm[i], sq[i], (i & 1) ? bfhi(xa[i >> 1]) : bflo(xa[i >> 1]), 0.f,        \
                          (i & 1) ? bfhi(xb[i >> 1]) : bflo(xb[i >> 1]), 0.f, 0.5f);               \
      else if (i < 12)                                                                             \
        wf_swap32(sm[i & 3], sq[i & 3], sm[i & 3], sq[i & 3], sm[(i & 3) + 4], sq[(i & 3) + 4], 1.f); \
      else if (i < 14)                                                                             \
        wf_swap16(sm[i & 1], sq[i & 1], sm[i & 1], sq[i & 1], sm[(i & 1) + 2], sq[(i & 1) + 2], 2.f); \
      else if (i == 14) {                                                                          \
        const bool hi8 = lane & 8;                                                                 \
        float mo = dppz<0x128>(hi8 ? sm[0] : sm[1]);                                               \
        float qo = dppz<0x128>(hi8 ? sq[0] : sq[1]);                                               \
        wf_merge(smm, sqq, hi8 ? sm[1] : sm[0], hi8 ? sq[1] : sq[0], mo, qo, 4.f);                 \
      } else if (i == 15) wf_dpp<0xB1>(smm, sqq, 8.f);                                             \
      else if (i == 16) wf_dpp<0x4E>(smm, sqq, 16.f);                                              \
      else if (i == 17) wf_dpp<0x141>(smm, sqq, 32.f);                                             \
      else if (i == 18) {                                                                          \
               \
        u32x2 v;                                                                                   \
        v[0] = __float_as_uint(smm);                                                               \
        v[1] = __float_as_uint(sqq);                                                               \
        u32 ad = lds0 + w * 4096 + 2048 + ((w & 1) ? 0 : 896) + lane;                              \
        asm volatile("s_mov_b64 exec, %2\n\tds_write_b64 %0, %1\n\ts_mov_b64 exec, -1"             \
                     ::"v"(ad), "v"(v), "s"(0x0101010101010101ull) : "memory");                    \
      }                                                                                            \
    }, {});                                                                                        \
    G1_IT(MODE, 1, 12, 1, {}, {                                                                    \
      if (w == 0) {                                                                                \
        u32x2 v;                                                                                   \
        int l1 = lane;                                                                             \
        asm volatile("" : "+v"(l1));                                                               \
        u32 ad = lds0 + (l1 >> 3) * 4096 + 2048 + ((l1 & 8) ? 0 : 896) + (l1 & 7) * 8;             \
        asm volatile("ds_read_b64 %0, %1\n\ts_waitcnt lgkmcnt(0)" : "=v"(v) : "v"(ad) : "memory"); \
        float mm = __uint_as_float(v[0]);                                                          \
        float qq = __uint_as_float(v[1]);                                                          \
        wf_dpp<0x128>(mm, qq, 64.f);                                                               \
        wf_swap16(mm, qq, mm, qq, mm, qq, 128.f);                                                  \
        wf_swap32(mm, qq, mm, qq, mm, qq, 256.f);                                                  \
        float r2 = rsqrtf(qq * (1.0f / D_) + 1e-5f);                                               \
        float rd = r2 * (mm - m1v);                                                                \
        u32 rb_ = __float_as_uint(r2);                                                             \
        if (rb_ == 0u) rb_ = 0x80000000u;                                                          \
        if (l1 < 8)                                                                                \
          __hip_atomic_store((gu64*)(st2 + r0 + l1),                                               \
                             ((unsigned long long)__float_as_uint(rd) << 32) | rb_,                \
                             __ATOMIC_RELAXED, __HIP_MEMORY_SCOPE_AGENT);                          \
      }                                                                                            \
      SCHED;                                                                                       \
    });                                                                                            \
    u32 pfd;                                                                                       \
    {                                                                                              \
            \
      const u32 po = (tid < 448 ? tid : 447) * 64;                                                 \
      asm volatile("global_load_dword %0, %1, %2"                                                  \
                   : "=v"(pfd) : "v"(po), "s"((const char*)w2 + (size_t)b * 28672) : "memory");    \
    }                                                                                              \
    TSC(2, 0);                                                                                     \
    for (int t = 6; t < 26; t += 2) {                                                              \
      G1_IT(MODE, 0, 12, 1, {}, {});                                                               \
      G1_IT(MODE, 1, 12, 1, {}, {});                                                               \
    }                                                                                              \
    TSC(2, 1);                                                                                     \
    asm volatile("" ::"v"(pfd));                                                                   \
    tid2 = tid;                                                                                    \
    asm volatile("" : "+v"(tid2));                                                                 \
    float cp[8];                                                                                   \
    G1_IT(MODE, 0, 12, 1, {}, {                                      \
      const u32 co = (tid2 < 448 ? tid2 : 447) * 4;                                                \
      _Pragma("unroll") for (int ks = 0; ks < 8; ++ks)                                             \
        asm volatile("global_load_dword %0, %1, %2"                                                \
                     : "=v"(cp[ks]) : "v"(co), "s"(c12 + (ks * 32 + nt) * 448) : "memory");        \
      SCHED;                                                                                       \
    });                                                                                            \
    sp = (gu64*)(st2 + s0 + wm * 64 + (tid2 & 15));                                                \
    G1_IT(MODE, 1, 20, 0, {}, {                                                                    \
      _Pragma("unroll") for (int m = 0; m < 4; ++m)                                                \
        asm volatile("global_load_dwordx2 %0, %1, off offset:%2 sc1"                               \
                     : "=v"(sg[m]) : "v"(sp), "i"(128 * m) : "memory");                            \
      SCHED;                                                                                       \
    });                                                                                            \
    G1_IT(MODE, 0, 20, 0, {}, {});                                                                 \
    G1_IT(MODE, 1, 8, 0, {}, {                       \
      asm volatile("" : "+v"(cp[0]), "+v"(cp[1]), "+v"(cp[2]), "+v"(cp[3]), "+v"(cp[4]),           \
                        "+v"(cp[5]), "+v"(cp[6]), "+v"(cp[7]) : : "memory");                       \
      float csum = ((cp[0] + cp[1]) + (cp[2] + cp[3])) + ((cp[4] + cp[5]) + (cp[6] + cp[7]));      \
      u32 ad = lds0 + 2 * G1_SLOT + tid2 * 4;                                                      \
      asm volatile("ds_write_b32 %0, %1" ::"v"(ad), "v"(csum) : "memory");                         \
      SCHED;                                                                                       \
    });                                                                                            \
    G1_IT(MODE, 0, 4, 0, {}, {});                                                                  \
    asm volatile("s_waitcnt vmcnt(0) lgkmcnt(0)"                                                   \
                 : "+v"(sg[0]), "+v"(sg[1]), "+v"(sg[2]), "+v"(sg[3]) : : "memory");               \
    SCHED;                                                                                         \
    _Pragma("unroll") for (int i = 0; i < 28; ++i) G1_MM(0, 1, i);                                   \
    asm volatile("s_nop 7\n\ts_nop 7" ::: "memory");                                               \
    SCHED;                                                                                         \
  } while (0)
  G1_MAIN(0);
  const int lane2 = tid2 & 63, g = lane2 >> 4;
  TS(3, 2);
  TSC(5, 1);

  {
    for (int spin = 0; spin < (1 << 22); ++spin) {
      bool ok = ((u32)sg[0] != 0u) & ((u32)sg[1] != 0u) & ((u32)sg[2] != 0u) & ((u32)sg[3] != 0u);
      if (__all(ok)) break;
      __builtin_amdgcn_s_sleep(8);
#pragma unroll
      for (int m = 0; m < 4; ++m) sg[m] = __hip_atomic_load(sp + 16 * m, __ATOMIC_RELAXED, __HIP_MEMORY_SCOPE_AGENT);
    }
    float r2[4], rd[4];
#pragma unroll
    for (int m = 0; m < 4; ++m) {
      r2[m] = __uint_as_float((u32)sg[m]);
      rd[m] = -__uint_as_float((u32)(sg[m] >> 32));
    }
    const float* cl = (const float*)(smem + 2 * G1_SLOT) + wn * 56;
    char* ob = (char*)hh + ((size_t)((s0 + wm * 64) >> 4) * 448 + nt * 14 + wn * 7) * 256 + (lane2 & 15) * 16;
#pragma unroll
    for (int j = 0; j < 3; ++j) {
      const f32x4 c1g = *(const f32x4*)(cl + 16 * j + 4 * g), c1u = *(const f32x4*)(cl + 112 + 16 * j + 4 * g);
      const f32x4 c2g = *(const f32x4*)(cl + 224 + 16 * j + 4 * g), c2u = *(const f32x4*)(cl + 336 + 16 * j + 4 * g);
      u32 o[4][2];
#pragma unroll
      for (int m = 0; m < 4; ++m) {
        float hv[4];
#pragma unroll
        for (int r = 0; r < 4; ++r) {
          float gt = fmaf(acc[2 * j][m][r], r2[m], fmaf(rd[m], c1g[r], c2g[r]));
          float up = fmaf(acc[2 * j + 1][m][r], r2[m], fmaf(rd[m], c1u[r], c2u[r]));
          hv[r] = silu_f(gt) * up;
        }
        o[m][0] = pk2(hv[0], hv[1]);
        o[m][1] = pk2(hv[2], hv[3]);
      }
#pragma unroll
      for (int mp = 0; mp < 2; ++mp) {
        auto d0 = __builtin_amdgcn_permlane16_swap(o[2 * mp][0], o[2 * mp + 1][0], false, false);
        auto d1 = __builtin_amdgcn_permlane16_swap(o[2 * mp][1], o[2 * mp + 1][1], false, false);
        u32x4 ov = {d0[0], d1[0], d0[1], d1[1]};
        ST16(ob + ((size_t)(2 * mp + (g & 1)) * 448 + 2 * j + (g >> 1)) * 256, ov, STPG);
      }
    }
    {
      const int co = (g >> 1) * 112 + 48 + 4 * (g & 1);
      const f32x4 c1 = *(const f32x4*)(cl + co), c2 = *(const f32x4*)(cl + 224 + co);
      u32 o[2][2];
#pragma unroll
      for (int mm = 0; mm < 2; ++mm) {
        float hv[4];
#pragma unroll
        for (int r = 0; r < 4; ++r) {
          float va = fmaf(acc[6][mm][r], r2[mm], fmaf(rd[mm], c1[r], c2[r]));
          float vb = fmaf(acc[6][mm + 2][r], r2[mm + 2], fmaf(rd[mm + 2], c1[r], c2[r]));
          auto sw = __builtin_amdgcn_permlane32_swap(__float_as_uint(va), __float_as_uint(vb), false, false);
          hv[r] = silu_f(__uint_as_float(sw[0])) * __uint_as_float(sw[1]);
        }
        o[mm][0] = pk2(hv[0], hv[1]);
        o[mm][1] = pk2(hv[2], hv[3]);
      }
      auto d0 = __builtin_amdgcn_permlane16_swap(o[0][0], o[1][0], false, false);
      auto d1 = __builtin_amdgcn_permlane16_swap(o[0][1], o[1][1], false, false);
      u32x4 ov = {d0[0], d1[0], d0[1], d1[1]};
      ST16(ob + ((size_t)g * 448 + 6) * 256, ov, STPG);
    }
  }
  TS(3, 3);
}

#define G2_BSLOT 4096
#define G2_XOFF 98304
#define G2_LDS (G2_XOFF + 16384)

__global__ __launch_bounds__(256, 1) void g2_gemm(const u16* __restrict__ hh, const u16* __restrict__ w2,
                                                  const u16* __restrict__ x1T, const float* __restrict__ modf,
                                                  u16* __restrict__ out) {
  extern __shared__ __attribute__((aligned(16))) char smem[];
  const int tid = threadIdx.x, lane = tid & 63;
  const int w = __builtin_amdgcn_readfirstlane(tid >> 6);
  const int b = blockIdx.x;
  const int xcd = b & 7, idx = b >> 3;
  const int mt = (xcd >> 1) * 4 + (idx & 3), nt = (xcd & 1) * 8 + (idx >> 2);
  const int s0 = mt * 128, d0 = nt * 64;
  char* wb = smem + w * (3 * G2_BSLOT);
  TS(4, 0);

#pragma unroll
  for (int i = 0; i < 4; ++i)
    GLDS16((const char*)x1T + (size_t)(d0 + 16 * i + (lane >> 2)) * (S_ * 2) + (s0 + 32 * w) * 2 + (lane & 3) * 16,
           smem + G2_XOFF + w * 4096 + i * 1024);

  u32 boff[2];
#pragma unroll
  for (int i = 0; i < 2; ++i) {
    int rho = 8 * i + (lane >> 3);
    int f = 8 * ((rho >> 2) & 3) + (rho & 3);
    boff[i] = (u32)(f * (D_ * 2) + (((lane & 7) ^ (((rho >> 1) & 3) << 1)) * 16));
  }
  const char* bbase = (const char*)w2 + (size_t)(32 * w) * (D_ * 2) + d0 * 2;
  const char* ab[8];
#pragma unroll
  for (int m = 0; m < 8; ++m)
    ab[m] = (const char*)hh + (size_t)((s0 >> 4) + ((m + 2 * w) & 7)) * (448 * 256);
  u32 aoff = w * 1024 + lane * 16;
#define G2_LDA(set, m) \
  asm volatile("global_load_dwordx4 %0, %1, %2" : "=v"(A[set][m]) : "v"(aoff), "s"(ab[m]) : "memory")
#define G2_LDB(slot, i) \
  GLDS16(bbase + (size_t)((i) >> 1) * (4 * D_ * 2) + boff[(i) & 1], wb + (slot) * G2_BSLOT + (i) * 1024)
#define G2_ADV do { aoff += 4096; bbase += (size_t)128 * D_ * 2; } while (0)

  const int g = lane >> 4, q = (lane >> 2) & 3, p = lane & 3;
  const u32 lds0 = (u32)(uintptr_t)(__attribute__((address_space(3))) char*)smem;
  u32 brd[4];
  {
    const int row = 4 * g + q;
#pragma unroll
    for (int n = 0; n < 4; ++n) {
      int c = (2 * n + (p >> 1)) ^ (((row >> 1) & 3) << 1);
      brd[n] = lds0 + w * (3 * G2_BSLOT) + row * 128 + c * 16 + 8 * (p & 1);
    }
  }
  f32x4 acc[4][8];
  u32x4 A[3][8];
  u32x2 bf[2][4][2];
#define G2_PRO(set)                                              \
  do {                                                           \
    _Pragma("unroll") for (int i = 0; i < 4; ++i) G2_LDB(set, i); \
    SCHED;                                                       \
    _Pragma("unroll") for (int m = 0; m < 8; ++m) G2_LDA(set, m); \
    SCHED;                                                       \
    G2_ADV;                                                      \
  } while (0)
#define G2_RDB(bs, slot)                                                   \
  do {                                                                     \
    _Pragma("unroll") for (int n = 0; n < 4; ++n) {                        \
      TRRD(bf[bs][n][0], brd[n], (slot) * G2_BSLOT);                       \
      TRRD(bf[bs][n][1], brd[n], (slot) * G2_BSLOT + 16 * 128);            \
    }                                                                      \
  } while (0)
  G2_PRO(0); G2_PRO(1); G2_PRO(2);
  WAIT_V(32);
  G2_RDB(0, 0);
#define G2_B4(bs, n) ((u32x4){bf[bs][n][0][0], bf[bs][n][0][1], bf[bs][n][1][0], bf[bs][n][1][1]})
#define G2_MFMA4                                          \
  "v_mfma_f32_16x16x32_bf16 %0, %5, %4, %0\n\t"           \
  "v_mfma_f32_16x16x32_bf16 %1, %6, %4, %1\n\t"           \
  "v_mfma_f32_16x16x32_bf16 %2, %7, %4, %2\n\t"           \
  "v_mfma_f32_16x16x32_bf16 %3, %8, %4, %3\n\t"
#define G2_MML(set, bs, m, WV)                                                                       \
  asm volatile("s_waitcnt vmcnt(%11)\n\t" G2_MFMA4 "global_load_dwordx4 %4, %9, %10"                 \
               : "+a"(acc[0][m]), "+a"(acc[1][m]), "+a"(acc[2][m]), "+a"(acc[3][m]), "+v"(A[set][m]) \
               : "v"(G2_B4(bs, 0)), "v"(G2_B4(bs, 1)), "v"(G2_B4(bs, 2)), "v"(G2_B4(bs, 3)),         \
                 "v"(aoff), "s"(ab[m]), "i"(WV)                                                      \
               : "memory")
#define G2_MML0(set, bs, m, WV)                                                                      \
  asm volatile("s_waitcnt vmcnt(%11)\n\t"                                                            \
               "v_mfma_f32_16x16x32_bf16 %0, %5, %4, 0\n\t"                                          \
               "v_mfma_f32_16x16x32_bf16 %1, %6, %4, 0\n\t"                                          \
               "v_mfma_f32_16x16x32_bf16 %2, %7, %4, 0\n\t"                                          \
               "v_mfma_f32_16x16x32_bf16 %3, %8, %4, 0\n\t"                                          \
               "global_load_dwordx4 %4, %9, %10"                                                     \
               : "=a"(acc[0][m]), "=a"(acc[1][m]), "=a"(acc[2][m]), "=a"(acc[3][m]), "+v"(A[set][m]) \
               : "v"(G2_B4(bs, 0)), "v"(G2_B4(bs, 1)), "v"(G2_B4(bs, 2)), "v"(G2_B4(bs, 3)),         \
                 "v"(aoff), "s"(ab[m]), "i"(WV)                                                      \
               : "memory")
#define G2_MM(set, bs, m, WV)                                                                        \
  asm volatile("s_waitcnt vmcnt(%9)\n\t" G2_MFMA4                                                    \
               : "+a"(acc[0][m]), "+a"(acc[1][m]), "+a"(acc[2][m]), "+a"(acc[3][m])                  \
               : "v"(A[set][m]), "v"(G2_B4(bs, 0)), "v"(G2_B4(bs, 1)), "v"(G2_B4(bs, 2)),            \
                 "v"(G2_B4(bs, 3)), "i"(WV)                                                          \
               : "memory")
#define G2_STEP(set, bs, MML)                                                                 \
  do {                                                                                        \
    WAIT_L(0);                                                                                \
    SCHED;                                                                                    \
    _Pragma("unroll") for (int i = 0; i < 4; ++i) G2_LDB(set, i);                             \
    SCHED;                                                                                    \
    _Pragma("unroll") for (int m = 0; m < 8; ++m) {                                           \
      MML(set, bs, m, 35);                                                                    \
      if (m == 3) { WAIT_V(28); G2_RDB((bs) ^ 1, ((set) + 1) % 3); }                          \
      SCHED;                                                                                  \
    }                                                                                         \
    G2_ADV;                                                                                   \
  } while (0)
#define G2_TAIL(set, bs, NEW, NEXT)                                                           \
  do {                                                                                        \
    WAIT_L(0);                                                                                \
    SCHED;                                                                                    \
    _Pragma("unroll") for (int m = 0; m < 8; ++m) {                                           \
      G2_MM(set, bs, m, (NEW) + 7 - m);                                                       \
      if (NEXT && m == 3) {                                                                   \
        asm volatile("s_waitcnt vmcnt(%0)" ::"i"((NEW) - 4) : "memory");                      \
        G2_RDB((bs) ^ 1, ((set) + 1) % 3);                                                    \
      }                                                                                       \
      SCHED;                                                                                  \
    }                                                                                         \
  } while (0)
  G2_STEP(0, 0, G2_MML0);
  for (int t = 0; t < 4; ++t) {
    G2_STEP(1, 1, G2_MML);
    G2_STEP(2, 0, G2_MML);
    G2_STEP(0, 1, G2_MML);
    G2_STEP(1, 0, G2_MML);
    G2_STEP(2, 1, G2_MML);
    G2_STEP(0, 0, G2_MML);
  }
  G2_TAIL(1, 1, 24, true);
  G2_TAIL(2, 0, 12, true);
  G2_TAIL(0, 1, 0, false);
  asm volatile("s_nop 7\n\ts_nop 7\n\ts_nop 7" ::: "memory");
#pragma unroll
  for (int m = 0; m < 8; m += 2)
    asm volatile("" : "+a"(acc[0][m]), "+a"(acc[1][m]), "+a"(acc[2][m]), "+a"(acc[3][m]), "+a"(acc[0][m + 1]),
                      "+a"(acc[1][m + 1]), "+a"(acc[2][m + 1]), "+a"(acc[3][m + 1]) : : "memory");
  f32x4 gbv[4];
#pragma unroll
  for (int n = 0; n < 4; ++n) gbv[n] = *(const f32x4*)(modf + d0 + 16 * n + 4 * g);
  SCHED;

  TS(4, 1);
  BAR;
  f32x4* rb = (f32x4*)smem + lane;
#pragma unroll
  for (int j = 1; j < 4; ++j) {
    f32x4* dst = rb + (((w + j) & 3) * 3 + (j - 1)) * 512;
#pragma unroll
    for (int mm = 0; mm < 2; ++mm)
#pragma unroll
      for (int n = 0; n < 4; ++n) dst[(mm * 4 + n) * 64] = acc[n][2 * j + mm];
  }
  SYNC_L;
  f32x4 fin[2][4];
#pragma unroll
  for (int mm = 0; mm < 2; ++mm)
#pragma unroll
    for (int n = 0; n < 4; ++n) {
      fin[mm][n] = acc[n][mm];
#pragma unroll
      for (int j = 0; j < 3; ++j) fin[mm][n] += rb[((w * 3 + j) * 8 + mm * 4 + n) * 64];
    }
  TS(4, 2);
  {
    const char* xb = smem + G2_XOFF + w * 4096 + (4 * g + q) * 64 + p * 8;
    char* ob = (char*)out + (size_t)(s0 + 32 * w + 16 * (g & 1) + (lane & 15)) * (D_ * 2) + (d0 + 8 * (g >> 1)) * 2;
#pragma unroll
    for (int n = 0; n < 4; ++n) {
      u32 o[2][2];
#pragma unroll
      for (int mm = 0; mm < 2; ++mm) {
        s16x4 xv = __builtin_amdgcn_ds_read_tr16_b64_v4i16((lds_s16x4p)(xb + n * 1024 + mm * 32));
        u32x2 xu = __builtin_bit_cast(u32x2, xv);
        o[mm][0] = pk2(fmaf(gbv[n][0], fin[mm][n][0], bflo(xu[0])), fmaf(gbv[n][1], fin[mm][n][1], bfhi(xu[0])));
        o[mm][1] = pk2(fmaf(gbv[n][2], fin[mm][n][2], bflo(xu[1])), fmaf(gbv[n][3], fin[mm][n][3], bfhi(xu[1])));
      }
      auto e0 = __builtin_amdgcn_permlane16_swap(o[0][0], o[1][0], false, false);
      auto e1 = __builtin_amdgcn_permlane16_swap(o[0][1], o[1][1], false, false);
      u32x4 ov = {e0[0], e1[0], e0[1], e1[1]};
      *(u32x4*)(ob + n * 32) = ov;
    }
  }
  TS(4, 3);
}

extern "C" void kernel_launch(void* const* d_in, const int* in_sizes, int n_in, void* d_out, int out_size,
                              void* d_ws, size_t ws_size, hipStream_t stream) {
  const u16* x = (const u16*)d_in[0];
  const u16* k = (const u16*)d_in[1];
  const u16* v = (const u16*)d_in[2];
  const u16* shk = (const u16*)d_in[3];
  const u16* ssmk = (const u16*)d_in[4];
  const u16* ssmD = (const u16*)d_in[5];
  const u16* w1 = (const u16*)d_in[7];
  const u16* w3 = (const u16*)d_in[8];
  const u16* w2 = (const u16*)d_in[9];
  const u16* c = (const u16*)d_in[10];
  const u16* wmod = (const u16*)d_in[11];
  char* ws = (char*)d_ws;
  u16* kvT = (u16*)(ws + WS_KVT);
  u16* xT = (u16*)(ws + WS_XT);
  u16* x1T = (u16*)(ws + WS_X1T);
  u16* apT = (u16*)(ws + WS_APT);
  u16* hh = (u16*)(ws + WS_HHT);
  float* modp = (float*)(ws + WS_MODP);
  f32x2* st1 = (f32x2*)(ws + WS_ST1);
  float* modf = (float*)(ws + WS_MOD);
  f32x2* st2 = (f32x2*)(ws + WS_ST2);
  float* c12 = (float*)(ws + WS_C12);

  (void)hipFuncSetAttribute((const void*)k1_conv, hipFuncAttributeMaxDynamicSharedMemorySize, K1_LDS);
  (void)hipFuncSetAttribute((const void*)g1_gemm, hipFuncAttributeMaxDynamicSharedMemorySize, G1_LDS);
  (void)hipFuncSetAttribute((const void*)g2_gemm, hipFuncAttributeMaxDynamicSharedMemorySize, G2_LDS);

  k0_prep<<<K0_TRB + K0_MODB + K0_STB, 256, 0, stream>>>(x, k, v, shk, c, wmod, kvT, xT, st1, modp, st2);
  k1_conv<<<256, 512, K1_LDS, stream>>>(ssmk, ssmD, kvT, xT, st1, modp, w1, w3, x1T, apT, modf, c12);
  g1_gemm<<<256, 512, G1_LDS, stream>>>(apT, w1, w3, x1T, st1, (unsigned long long*)st2, c12, w2, hh);
  g2_gemm<<<256, 256, G2_LDS, stream>>>(hh, w2, x1T, modf, (u16*)d_out);
}
```

```cpp
#include <hip/hip_runtime.h>
#include <hip/hip_bf16.h>
#include <stdint.h>

typedef unsigned short u16;
typedef unsigned int u32;
typedef __attribute__((ext_vector_type(8))) __bf16 bf16x8;
typedef __attribute__((ext_vector_type(2))) __bf16 bf16x2;
typedef __attribute__((ext_vector_type(4))) short s16x4;
typedef __attribute__((ext_vector_type(8))) short s16x8;
typedef __attribute__((ext_vector_type(4))) float f32x4;
typedef __attribute__((ext_vector_type(2))) float f32x2;
typedef __attribute__((ext_vector_type(4))) u32 u32x4;
typedef __attribute__((ext_vector_type(2))) u32 u32x2;

#define SLAB_EARLY 0
#define K1_REMAP 1
#define S_ 2048
#define D_ 1024
#define F_ 3584

#define WS_KVT (0u)
#define WS_XT (4u << 20)
#define WS_X1T (8u << 20)
#define WS_APT (12u << 20)
#define WS_HHT (16u << 20)
#define WS_MODP (32u << 20)
#define WS_ST1 (33u << 20)
#define WS_MOD ((33u << 20) + (64u << 10))
#define WS_ST2 ((33u << 20) + (128u << 10))
#define WS_C12 ((33u << 20) + (192u << 10))
#define WS_XCH (40u << 20)
#define XST "sc1"
#define XLD "sc1"
#define XFL "sc1"
#define G2_SLEEP 8

__device__ __forceinline__ float bflo(u32 u) { return __uint_as_float(u << 16); }
__device__ __forceinline__ float bfhi(u32 u) { return __uint_as_float(u & 0xffff0000u); }
__device__ __forceinline__ float bf1(u16 h) { return __uint_as_float(((u32)h) << 16); }
__device__ __forceinline__ u32 pk2(float a, float b) {
  bf16x2 r = {(__bf16)a, (__bf16)b};
  return __builtin_bit_cast(u32, r);
}
__device__ __forceinline__ float rbf(float a) {
  return __uint_as_float(((u32)__builtin_bit_cast(u16, (__bf16)a)) << 16);
}
__device__ __forceinline__ float silu_f(float g) { return g * __builtin_amdgcn_rcpf(1.0f + __expf(-g)); }

#define GLDS16(gptr, ldsptr)                                                        \
  __builtin_amdgcn_global_load_lds((const __attribute__((address_space(1))) u32*)(gptr), \
                                   (__attribute__((address_space(3))) u32*)(ldsptr), 16, 0, 0)
#define WAIT_V(n) asm volatile("s_waitcnt vmcnt(" #n ")" ::: "memory")
#define WAIT_L(n) asm volatile("s_waitcnt lgkmcnt(" #n ")" ::: "memory")
#define BAR __builtin_amdgcn_s_barrier()
#define SCHED __builtin_amdgcn_sched_barrier(0)
#define STP0 ""
#define STP1 "sc1"
#define STPG "sc1"
#define ST16(ptr, val, POL) asm volatile("global_store_dwordx4 %0, %1, off " POL "\n\ts_nop 1" ::"v"(ptr), "v"(val) : "memory")
#define ST8(ptr, val, POL) asm volatile("global_store_dwordx2 %0, %1, off " POL ::"v"(ptr), "v"(val) : "memory")

#define TS(k, i)
#define TSC(k, i)
#define TSW(k, i, wv)

__device__ __forceinline__ float wave_sum(float v) {
#pragma unroll
  for (int o = 32; o > 0; o >>= 1) v += __shfl_xor(v, o, 64);
  return v;
}

#define K0_MODB 384
#define K0_STB 256
#define K0_TRB 256
__global__ __launch_bounds__(256) void k0_prep(const u16* __restrict__ x, const u16* __restrict__ kk,
                                               const u16* __restrict__ vv, const u16* __restrict__ shk,
                                               const u16* __restrict__ cvec, const u16* __restrict__ wmod,
                                               u16* __restrict__ kvT, u16* __restrict__ xT,
                                               f32x2* __restrict__ st1, float* __restrict__ modp,
                                               f32x2* __restrict__ st2) {
  __shared__ float red[8 * 256];
  const int tid = threadIdx.x;
  int b = blockIdx.x;
  TS(0, 0);
  if (b < K0_TRB) {
    const int dt = b & 15, stile = b >> 4;
    const int lane = tid & 63, w = tid >> 6;
    const int c8 = lane & 7, sq = lane >> 3;
    const int s0 = stile * 128 + w * 32 + sq * 4;
    const int d0 = dt * 64 + c8 * 8;
    u32x4 kr[7], vr[4], xr[4], sw[4];
#pragma unroll
    for (int i = 0; i < 7; ++i) {
      int s = s0 - 3 + i;
      int sc = s < 0 ? 0 : s;
      kr[i] = *(const u32x4*)(kk + (size_t)sc * D_ + d0);
    }
#pragma unroll
    for (int i = 0; i < 4; ++i) vr[i] = *(const u32x4*)(vv + (size_t)(s0 + i) * D_ + d0);
#pragma unroll
    for (int i = 0; i < 4; ++i) xr[i] = *(const u32x4*)(x + (size_t)(s0 + i) * D_ + d0);
#pragma unroll
    for (int i = 0; i < 4; ++i) sw[i] = *(const u32x4*)(shk + (size_t)d0 * 4 + i * 8);
    if (s0 == 0) {
#pragma unroll
      for (int i = 0; i < 3; ++i) kr[i] = (u32x4){0, 0, 0, 0};
    }
#pragma unroll
    for (int j = 0; j < 8; ++j) {
      u32 wa = sw[j >> 1][(j & 1) * 2], wb = sw[j >> 1][(j & 1) * 2 + 1];
      float w0 = bflo(wa), w1 = bfhi(wa), w2 = bflo(wb), w3 = bfhi(wb);
      float kf[7];
#pragma unroll
      for (int i = 0; i < 7; ++i) kf[i] = (j & 1) ? bfhi(kr[i][j >> 1]) : bflo(kr[i][j >> 1]);
      float o[4];
#pragma unroll
      for (int i = 0; i < 4; ++i) {
        float ks = kf[i] * w3;
        ks = fmaf(kf[i + 1], w2, ks);
        ks = fmaf(kf[i + 2], w1, ks);
        ks = fmaf(kf[i + 3], w0, ks);
        float vf = (j & 1) ? bfhi(vr[i][j >> 1]) : bflo(vr[i][j >> 1]);
        o[i] = ks * vf;
      }
      u32x2 ov = {pk2(o[0], o[1]), pk2(o[2], o[3])};
      ST8(kvT + (size_t)(d0 + j) * S_ + s0, ov, STP0);
      const u32 sel = (j & 1) ? 0x07060302u : 0x05040100u;
      u32x2 xv = {__builtin_amdgcn_perm(xr[1][j >> 1], xr[0][j >> 1], sel),
                  __builtin_amdgcn_perm(xr[3][j >> 1], xr[2][j >> 1], sel)};
      ST8(xT + (size_t)(d0 + j) * S_ + s0, xv, STP0);
    }
    TS(0, 3);
    return;
  }
  b -= K0_TRB;
  if (b < K0_MODB) {
    const int ks = b / 24, strip = b % 24;
    const int cl = tid & 31, rg = tid >> 5;
    u32x4 wv[8];
    u16 cv[8];
#pragma unroll
    for (int i = 0; i < 8; ++i) {
      int row = ks * 64 + rg + 8 * i;
      wv[i] = *(const u32x4*)(wmod + (size_t)row * (6 * D_) + strip * 256 + cl * 8);
      cv[i] = cvec[row];
    }
    float acc[8];
#pragma unroll
    for (int j = 0; j < 8; ++j) acc[j] = 0.f;
#pragma unroll
    for (int i = 0; i < 8; ++i) {
      float cf = bf1(cv[i]);
      float sc = rbf(cf / (1.0f + __expf(-cf)));
#pragma unroll
      for (int j = 0; j < 4; ++j) {
        acc[2 * j] = fmaf(sc, bflo(wv[i][j]), acc[2 * j]);
        acc[2 * j + 1] = fmaf(sc, bfhi(wv[i][j]), acc[2 * j + 1]);
      }
    }
    *(f32x4*)&red[rg * 256 + cl * 8] = (f32x4){acc[0], acc[1], acc[2], acc[3]};
    *(f32x4*)&red[rg * 256 + cl * 8 + 4] = (f32x4){acc[4], acc[5], acc[6], acc[7]};
    __syncthreads();
    float s = 0.f;
#pragma unroll
    for (int i = 0; i < 8; ++i) s += red[i * 256 + tid];
    modp[ks * (6 * D_) + strip * 256 + tid] = s;
    TS(0, 3);
    return;
  }
  b -= K0_MODB;
  {
    const int lane = tid & 63, w = tid >> 6;
    const int r0 = b * 8 + w * 2;
    u32x4 a[2][2];
#pragma unroll
    for (int r = 0; r < 2; ++r)
#pragma unroll
      for (int h = 0; h < 2; ++h) a[r][h] = *(const u32x4*)(x + (size_t)(r0 + r) * D_ + h * 512 + lane * 8);
#pragma unroll
    for (int r = 0; r < 2; ++r) {
      float s = 0.f;
#pragma unroll
      for (int h = 0; h < 2; ++h)
#pragma unroll
        for (int j = 0; j < 4; ++j) s += bflo(a[r][h][j]) + bfhi(a[r][h][j]);
      s = wave_sum(s);
      float m = s * (1.0f / D_);
      float q = 0.f;
#pragma unroll
      for (int h = 0; h < 2; ++h)
#pragma unroll
        for (int j = 0; j < 4; ++j) {
          float d0 = bflo(a[r][h][j]) - m, d1 = bfhi(a[r][h][j]) - m;
          q = fmaf(d0, d0, q);
          q = fmaf(d1, d1, q);
        }
      q = wave_sum(q);
      float rs = rsqrtf(q * (1.0f / D_) + 1e-5f);
      if (lane == 0) {
        st1[r0 + r] = (f32x2){m, rs};
        st2[r0 + r] = (f32x2){0.f, 0.f};
      }
    }
    TS(0, 3);
  }
}

#define K1_KOFF 0
#define K1_UOFF 4224
#define K1_XOFF 8576
#define K1_YOFF 12672
#define K1_COFF 20864
#define K1_WSZ 21408
#define K1_STOFF (4 * K1_WSZ)
#define K1_SLOFF (K1_STOFF + 16384)
#define K1_MVOFF (K1_SLOFF + 57344)
#define K1_LDS (K1_MVOFF + 512)

typedef __attribute__((address_space(3))) s16x4* lds_s16x4p;
__device__ __forceinline__ bf16x8 tr_frag(const char* p0, const char* p1) {
  s16x4 a = __builtin_amdgcn_ds_read_tr16_b64_v4i16((lds_s16x4p)p0);
  s16x4 b = __builtin_amdgcn_ds_read_tr16_b64_v4i16((lds_s16x4p)p1);
  s16x8 c = __builtin_shufflevector(a, b, 0, 1, 2, 3, 4, 5, 6, 7);
  return __builtin_bit_cast(bf16x8, c);
}
template <int CTRL>
__device__ __forceinline__ float dppz(float v) {
  return __builtin_bit_cast(float, __builtin_amdgcn_update_dpp(0, __builtin_bit_cast(int, v), CTRL, 0xf, 0xf, true));
}
#define TRRD(dst, addr, off) \
  asm volatile("ds_read_b64_tr_b16 %0, %1 offset:%2" : "=v"(dst) : "v"(addr), "i"(off))
#define SYNC_ALL asm volatile("s_waitcnt vmcnt(0) lgkmcnt(0)\n\ts_barrier" ::: "memory")
#define SYNC_L asm volatile("s_waitcnt lgkmcnt(0)\n\ts_barrier" ::: "memory")

__global__ __launch_bounds__(512, 2) void k1_conv(const u16* __restrict__ ssmk, const u16* __restrict__ ssmD,
                                                  const u16* __restrict__ kvT, const u16* __restrict__ xT,
                                                  const f32x2* __restrict__ st1, const float* __restrict__ modp,
                                                  const u16* __restrict__ w1, const u16* __restrict__ w3,
                                                  u16* __restrict__ x1T, u16* __restrict__ apT,
                                                  float* __restrict__ modf, float* __restrict__ c12) {
  extern __shared__ __attribute__((aligned(16))) char smem[];
  const int tid = threadIdx.x, lane = tid & 63;
  const int w = __builtin_amdgcn_readfirstlane(tid >> 6);
  TS(1, 0);
  const int c = w & 3, h = w >> 2;
  const int ch = ((blockIdx.x & 7) + 8 * (blockIdx.x >> 7)) * 64 + ((blockIdx.x >> 3) & 15) * 4 + c;
  char* wb = smem + c * K1_WSZ;
  {
    const u16* src = (h ? kvT : ssmk) + (size_t)ch * S_ + lane * 8;
    char* dst = wb + (h ? K1_UOFF + 256 : K1_KOFF + 128);
#pragma unroll
    for (int i = 0; i < 4; ++i) GLDS16(src + i * 512, dst + i * 1024);
    SCHED;
#pragma unroll
    for (int i = 0; i < 2; ++i) {
      GLDS16(xT + (size_t)ch * S_ + (2 * h + i) * 512 + lane * 8, wb + K1_XOFF + (2 * h + i) * 1024);
      GLDS16((const char*)st1 + (w * 2 + i) * 1024 + lane * 16, smem + K1_STOFF + (w * 2 + i) * 1024);
    }
  }
  SCHED;
  float mv0, mv1;
  u32 ddu;
  {
    const u32 o0 = ((lane & 15) * (6 * D_) + (lane >> 4) * D_) * 4;
    const u32 o1 = ((lane & 15) * (6 * D_) + (4 + ((lane >> 4) & 1)) * D_) * 4;
    asm volatile("global_load_dword %0, %1, %2" : "=v"(mv0) : "v"(o0), "s"(modp + ch) : "memory");
    asm volatile("global_load_dword %0, %1, %2" : "=v"(mv1) : "v"(o1), "s"(modp + ch) : "memory");
    asm volatile("global_load_ushort %0, %1, %2" : "=v"(ddu) : "v"(0u), "s"(ssmD + ch) : "memory");
  }
  const int nt = (blockIdx.x & 7) * 4 + ((blockIdx.x >> 3) & 3), ks = blockIdx.x >> 5;
  f32x4 ms0, ms1;
  {
    const u32 o = ((lane & 15) * (6 * D_) + (lane >> 4) * 4) * 4;
    const float* mb = modp + 3 * D_ + ks * 128 + w * 16;
    asm volatile("global_load_dwordx4 %0, %1, %2" : "=v"(ms0) : "v"(o), "s"(mb + D_) : "memory");
    asm volatile("global_load_dwordx4 %0, %1, %2" : "=v"(ms1) : "v"(o), "s"(mb) : "memory");
  }
  SCHED;
#define K1_SLAB(i)                                                                                   \
  do {                                                                                               \
    const int u_ = (i) * 64 + lane;                                                                  \
    const int um_ = u_ >= 224 ? u_ - 224 : u_;                                                       \
    const u16* wp_ = (u_ >= 224 ? w3 : w1) + (size_t)(ks * 128 + w * 16) * F_ + nt * 112;            \
    GLDS16((const char*)wp_ + (u32)((um_ / 14) * (F_ * 2) + (um_ % 14) * 16),                        \
           smem + K1_SLOFF + w * 7168 + (i) * 1024);                                                 \
  } while (0)
  const u32 lwb = (u32)(uintptr_t)(__attribute__((address_space(3))) char*)wb;
  {
    const u32 za = lwb + (h ? K1_UOFF + (lane & 15) * 16 : ((lane & 15) < 8 ? K1_KOFF + (lane & 7) * 16 : K1_COFF));
    const u32x4 z4 = {0, 0, 0, 0};
    if (lane < 16) asm volatile("ds_write_b128 %0, %1" ::"v"(za), "v"(z4) : "memory");
  }
  const int G = lane >> 4, q = (lane >> 2) & 3, p = lane & 3;
  const u32 abase = lwb + K1_KOFF + 128 - 96 + 32 * p + 8 * (8 * G + q);
  const u32 bbase = lwb + K1_UOFF + 256 + 96 - 32 + 8 * p - 8 * (8 * G + q) + 128 * h;
  const u32 yb = lwb + K1_YOFF + 4 * (64 * h + lane);
  const u32 cy = lwb + K1_COFF + 16 * (h + 1);
  const u32 lds0 = (u32)(uintptr_t)(__attribute__((address_space(3))) char*)smem;
  const u32 mva = lds0 + K1_MVOFF + (w >> 1) * 128 + G * 16 + (w & 1) * 8;
  asm volatile("s_waitcnt vmcnt(9) lgkmcnt(0)\n\ts_barrier" ::: "memory");
  TS(1, 1);
  TSC(5, 2);

  u32x2 Af[16][2], Bf[16][2];
#define K1_RD(a)                               \
  do {                                         \
    TRRD(Af[a][0], abase, 256 * (a));          \
    TRRD(Af[a][1], abase, 256 * (a) + 32);     \
    TRRD(Bf[a][0], bbase, 256 * (a) + 32);     \
    TRRD(Bf[a][1], bbase, 256 * (a));          \
  } while (0)
  K1_RD(0); K1_RD(1); K1_RD(2);
  f32x4 zp = {0, 0, 0, 0};
  float sa, ca1, ga, cb1, dd;
#pragma unroll
  for (int a = 0; a < 17; ++a) {
    if (a < 16) {
      const int n = a < 2 ? 8 : a == 2 ? 10 : a == 3 ? 12 : a < 14 ? 14 : a == 14 ? 10 : 6;
      asm volatile("s_waitcnt lgkmcnt(%0)" ::"i"(n) : "memory");
    }
    SCHED;
    if (a + 3 < 16) K1_RD(a + 3);
    if (a >= 1 && a < 8) { K1_SLAB(a - 1); SCHED; }
    f32x4 z = {0, 0, 0, 0};
    if (a < 16) {
#pragma unroll
      for (int i = 0; i <= a; ++i) {
        u32x4 a4 = {Af[i][0][0], Af[i][0][1], Af[i][1][0], Af[i][1][1]};
        u32x4 b4 = {Bf[a - i][0][0], Bf[a - i][0][1], Bf[a - i][1][0], Bf[a - i][1][1]};
        z = __builtin_amdgcn_mfma_f32_16x16x32_bf16(__builtin_bit_cast(bf16x8, a4), __builtin_bit_cast(bf16x8, b4),
                                                    z, 0, 0, 0);
      }
    }
    if (a > 0) {
      float w2 = dppz<0x138>(zp[3]) + zp[2];
      float w1 = dppz<0x138>(w2) + zp[1];
      float y = dppz<0x138>(w1) + zp[0];
      f32x4 cv = {w1, w2, zp[3], 0.f};
      asm volatile("ds_write_b32 %0, %1 offset:%2" ::"v"(yb), "v"(y), "i"(512 * (a - 1)) : "memory");
      asm volatile("s_mov_b64 exec, %3\n\tds_write_b128 %0, %1 offset:%2\n\ts_mov_b64 exec, -1"
                   ::"v"(cy), "v"(cv), "i"(32 * (a - 1)), "s"(0x8000000000000000ull) : "memory");
    }
    if (a == 8) TSC(5, 5);
    if (a == 12) TSC(5, 6);
    if (a == 12) {
      asm volatile("s_waitcnt vmcnt(7)" : "+v"(mv0), "+v"(mv1), "+v"(ddu), "+v"(ms0), "+v"(ms1) : : "memory");
#define RSUM16(v) do { v += dppz<0xB1>(v); v += dppz<0x4E>(v); v += dppz<0x141>(v); v += dppz<0x140>(v); } while (0)
      RSUM16(mv0);
      RSUM16(mv1);
      mv0 = rbf(mv0);
      mv1 = rbf(mv1);
      const float m0p = rbf(1.0f + mv0), m1p = rbf(1.0f + mv1);
#define RDL(v, l) __builtin_bit_cast(float, __builtin_amdgcn_readlane(__builtin_bit_cast(int, v), l))
      sa = RDL(mv0, 0); ca1 = RDL(m0p, 16); ga = RDL(mv0, 32); cb1 = RDL(m1p, 0);
      dd = bflo(ddu);
      if (h == 0 && lane == 16) modf[ch] = mv1;
    }
    if (a == 13 || a == 14) {
      f32x4 m = a == 13 ? ms0 : ms1;
#pragma unroll
      for (int r = 0; r < 4; ++r) {
        float t = m[r];
        RSUM16(t);
        if (a == 13) t = 1.0f + rbf(t);
        m[r] = t;
      }
      u32x2 mv = {pk2(m[0], m[1]), pk2(m[2], m[3])};
      asm volatile("s_mov_b64 exec, %3\n\tds_write_b64 %0, %1 offset:%2\n\ts_mov_b64 exec, -1"
                   ::"v"(mva), "v"(mv), "i"(64 * (a - 13)), "s"(0x0001000100010001ull) : "memory");
    }
    zp = z;
    SCHED;
  }
  TSC(5, 3);
  SYNC_L;
  TS(1, 2);
  TSC(5, 4);
#pragma unroll
  for (int it = 0; it < 2; ++it) {
    const int so = h * 1024 + it * 512 + lane * 8;
    f32x4 y0 = *(const f32x4*)(wb + K1_YOFF + so * 4);
    f32x4 y1 = *(const f32x4*)(wb + K1_YOFF + so * 4 + 16);
    f32x4 cv = *(const f32x4*)(wb + K1_COFF + ((lane & 7) ? 0 : (so >> 6) * 16));
    u32x4 uv = *(const u32x4*)(wb + K1_UOFF + 256 + so * 2);
    u32x4 xv = *(const u32x4*)(wb + K1_XOFF + so * 2);
    y0 += cv;
    u32x4 o1, o2;
#pragma unroll
    for (int j = 0; j < 4; ++j) {
      f32x4 st = *(const f32x4*)(smem + K1_STOFF + (so + 2 * j) * 8);
      float r[2], ap[2];
#pragma unroll
      for (int e = 0; e < 2; ++e) {
        float xf = e ? bfhi(xv[j]) : bflo(xv[j]);
        float yf = j < 2 ? y0[2 * j + e] : y1[2 * j - 4 + e];
        float uf = e ? bfhi(uv[j]) : bflo(uv[j]);
        float m1 = st[2 * e], r1 = st[2 * e + 1];
        float h1 = fmaf((xf - m1) * r1, ca1, sa);
        float yy = fmaf(uf, dd, yf);
        r[e] = fmaf(ga, h1 * yy, xf);
      }
      o1[j] = pk2(r[0], r[1]);
      ap[0] = (bflo(o1[j]) - st[0]) * cb1;
      ap[1] = (bfhi(o1[j]) - st[2]) * cb1;
      o2[j] = pk2(ap[0], ap[1]);
    }
    ST16(x1T + (size_t)ch * S_ + so, o1, STP1);
    ST16(apT + (size_t)ch * S_ + so, o2, STP1);
  }
  TS(1, 3);
  SCHED;
  asm volatile("s_waitcnt vmcnt(4)\n\ts_barrier" ::: "memory");
  {
    const int f1 = w < 6 ? w + 8 : w;
    const u32 ra = lds0 + K1_SLOFF + (4 * G + q) * 224 + p * 8;
    const u32 a0 = ra + (w >= 7 ? 3584 + (w - 7) * 32 : w * 32);
    const u32 a1 = ra + (f1 >= 7 ? 3584 + (f1 - 7) * 32 : f1 * 32);
    const u32 mb = lds0 + K1_MVOFF + (lane & 1) * 64 + G * 16;
    u32x2 fa[4][2], fb[4][2];
    u32x4 mf[4];
#pragma unroll
    for (int k = 0; k < 4; ++k) {
      asm volatile("ds_read_b128 %0, %1 offset:%2" : "=v"(mf[k]) : "v"(mb), "i"(128 * k));
      TRRD(fa[k][0], a0, 14336 * k);
      TRRD(fa[k][1], a0, 14336 * k + 7168);
      TRRD(fb[k][0], a1, 14336 * k);
      TRRD(fb[k][1], a1, 14336 * k + 7168);
    }
    WAIT_L(0);
    SCHED;
    f32x4 z0 = {0, 0, 0, 0}, z1 = {0, 0, 0, 0};
#pragma unroll
    for (int k = 0; k < 4; ++k) {
      u32x4 x0 = {fa[k][0][0], fa[k][0][1], fa[k][1][0], fa[k][1][1]};
      u32x4 x1 = {fb[k][0][0], fb[k][0][1], fb[k][1][0], fb[k][1][1]};
      z0 = __builtin_amdgcn_mfma_f32_16x16x32_bf16(__builtin_bit_cast(bf16x8, x0), __builtin_bit_cast(bf16x8, mf[k]), z0, 0, 0, 0);
      z1 = __builtin_amdgcn_mfma_f32_16x16x32_bf16(__builtin_bit_cast(bf16x8, x1), __builtin_bit_cast(bf16x8, mf[k]), z1, 0, 0, 0);
    }
    if ((lane & 15) < 2) {
      float* cp = c12 + (ks * 32 + nt) * 448 + (lane & 1) * 224 + 4 * G;
      *(f32x4*)(cp + 16 * w) = z0;
      if (w < 6) *(f32x4*)(cp + 16 * f1) = z1;
    }
  }
  TS(1, 4);
}

#define ABL_BAR "s_barrier"
#define ABL_RD 22
#define ABL_G 1
#define GP0 22
#define GPS 1
#define G1_SLOT 32768
#define G1_R 5
#define G1_BOFF 16384
#define G1_LDS (G1_R * G1_SLOT)

typedef __attribute__((address_space(1))) unsigned long long gu64;

__device__ __forceinline__ void wf_merge(float& m, float& q, float ma, float qa, float mb, float qb, float hn) {
  float d = mb - ma;
  m = 0.5f * (ma + mb);
  q = fmaf(d * hn, d, qa + qb);
}
__device__ __forceinline__ void wf_swap32(float& m, float& q, float ma, float qa, float mb, float qb, float hn) {
  auto a = __builtin_amdgcn_permlane32_swap(__float_as_uint(ma), __float_as_uint(mb), false, false);
  auto c = __builtin_amdgcn_permlane32_swap(__float_as_uint(qa), __float_as_uint(qb), false, false);
  wf_merge(m, q, __uint_as_float(a[0]), __uint_as_float(c[0]), __uint_as_float(a[1]), __uint_as_float(c[1]), hn);
}
__device__ __forceinline__ void wf_swap16(float& m, float& q, float ma, float qa, float mb, float qb, float hn) {
  auto a = __builtin_amdgcn_permlane16_swap(__float_as_uint(ma), __float_as_uint(mb), false, false);
  auto c = __builtin_amdgcn_permlane16_swap(__float_as_uint(qa), __float_as_uint(qb), false, false);
  wf_merge(m, q, __uint_as_float(a[0]), __uint_as_float(c[0]), __uint_as_float(a[1]), __uint_as_float(c[1]), hn);
}
template <int CTRL>
__device__ __forceinline__ void wf_dpp(float& m, float& q, float hn) {
  float mo = dppz<CTRL>(m), qo = dppz<CTRL>(q);
  wf_merge(m, q, m, q, mo, qo, hn);
}

__global__ __launch_bounds__(512, 2) void g1_gemm(const u16* __restrict__ apT, const u16* __restrict__ w1,
                                                  const u16* __restrict__ w3, const u16* __restrict__ x1T,
                                                  const f32x2* __restrict__ st1, unsigned long long* st2,
                                                  const float* __restrict__ c12, const u16* __restrict__ w2,
                                                  char* xch, u16* __restrict__ hh) {
  extern __shared__ __attribute__((aligned(16))) char smem[];
  const int tid = threadIdx.x, lane = tid & 63;
  const int w = __builtin_amdgcn_readfirstlane(tid >> 6);
  const int grp = w >> 2;
  const int wm = w & 3, wn = grp;
  const int b = blockIdx.x;
  const int xcd = b & 7, idx = b >> 3;
  const int mt = idx & 7, nt = xcd * 4 + (idx >> 3);
  const int s0 = mt * 256, f0 = nt * 112;
  TS(3, 0);

  const u32 lds0 = (u32)(uintptr_t)(__attribute__((address_space(3))) char*)smem;
  u32 voa[2], vob;
#pragma unroll
  for (int j = 0; j < 2; ++j) {
    int row = 4 * w + 2 * j + (lane >> 5);
    int c = (lane & 31) ^ ((row & 7) << 1);
    voa[j] = row * (S_ * 2) + c * 16 - j * 1024;
  }
  {
    int li = lane - 8 * (w & 1);
    li = li < 0 ? 0 : (li > 55 ? 55 : li);
    vob = (4 * w + li / 14) * (F_ * 2) + (li % 14) * 16;
  }
  const unsigned long long bmask = (w & 1) ? 0xFFFFFFFFFFFFFF00ull : 0x00FFFFFFFFFFFFFFull;
  const char* sa_ = (const char*)apT + s0 * 2;
  const char* sb1 = (const char*)w1 + f0 * 2 - 2048;
  const char* sb3 = (const char*)w3 + f0 * 2 - 3072;
  u32 wdst = lds0 + w * 4096;
#define G1_GA(j)                                                                                   \
  asm volatile("s_mov_b32 m0, %0\n\ts_nop 0\n\tglobal_load_lds_dwordx4 %1, %2 offset:%3"           \
               ::"s"(wdst), "v"(voa[j]), "s"(sa_), "i"((j) * 1024) : "memory")
#define GBX "s_mov_b32 m0, %0\n\ts_mov_b64 exec, %4\n\tglobal_load_lds_dwordx4 %1, %2 offset:%3\n\ts_mov_b64 exec, -1"
#define G1_GB(sb, off)                                                                             \
  asm volatile(GBX \
               ::"s"(wdst), "v"(vob), "s"(sb), "i"(off), "s"(bmask) : "memory")
#define G1_ADV                                                                                     \
  do {                                                                                             \
    sa_ += 32 * S_ * 2; sb1 += 32 * F_ * 2; sb3 += 32 * F_ * 2;                                    \
    wdst += G1_SLOT; if (wdst >= lds0 + G1_R * G1_SLOT) wdst -= G1_R * G1_SLOT;                    \
  } while (0)
#pragma unroll
  for (int i = 0; i < G1_R; ++i) {
    G1_GB(sb1, 2048); G1_GB(sb3, 3072); G1_GA(0); G1_GA(1);
    G1_ADV;
  }
  SCHED;

  const int r0 = (xcd * 32 + idx) * 8;
  u32x4 xa, xb;
  float m1v;
  {
    const u32 xo = tid * (S_ * 2);
    const char* xbase0 = (const char*)x1T + r0 * 2;
    const char* xbase1 = xbase0 + 512 * (S_ * 2);
    asm volatile("global_load_dwordx4 %0, %1, %2" : "=v"(xa) : "v"(xo), "s"(xbase0) : "memory");
    asm volatile("global_load_dwordx4 %0, %1, %2" : "=v"(xb) : "v"(xo), "s"(xbase1) : "memory");
    const u32 mo = (lane & 7) * 8;
    asm volatile("global_load_dword %0, %1, %2" : "=v"(m1v) : "v"(mo), "s"(st1 + r0) : "memory");
  }
  SCHED;

  u32 ard[4], brd[2];
  {
    const int g = lane >> 4, q = (lane >> 2) & 3, p = lane & 3;
    const int row = 4 * g + q;
#pragma unroll
    for (int m = 0; m < 4; ++m) {
      int c = (8 * wm + 2 * m + (p >> 1)) ^ ((row & 7) << 1);
      ard[m] = lds0 + g * 4096 + q * 512 + c * 16 + 8 * (p & 1);
    }
    const u32 bb = lds0 + g * 4096 + 2048 + (g & 1) * 128 + q * 224 + 7 * wn * 16 + 8 * (p & 1);
    brd[0] = bb + (p >> 1) * 16;
    brd[1] = bb + (p >> 1) * 1024 + 96;
  }

  f32x4 acc[7][4];
  u32x2 fa[2][4][2], fb[2][7][2];
  u32 rso = 0;
  u32 ra[4], rb[2];
#define G1_RDX(TR, st, i)                                                                            \
  do {                                                                                             \
    if ((i) < 14) {                                                                                \
      const int n_ = (i) >> 1;                                                                     \
      TR(fb[st][n_][(i) & 1], rb[n_ < 6 ? 0 : 1],                                                \
           (n_ < 6 ? (n_ & 1) * 1024 + (n_ >> 1) * 32 : 0) + ((i) & 1) * 16384);                   \
    } else {                                                                                       \
      TR(fa[st][((i) - 14) >> 1][(i) & 1], ra[((i) - 14) >> 1], ((i) & 1) * 16384);              \
    }                                                                                              \
  } while (0)
#define G1_RD(st, i) G1_RDX(TRRD, st, i)
#define TRRD2(dst, addr, off) \
  asm volatile("ds_read_b64_tr_b16 %0, %1 offset:%2" : "+v"(dst) : "v"(addr), "i"(off))
#define G1_RADDR                                                                                   \
  do {                                                                                             \
    _Pragma("unroll") for (int m = 0; m < 4; ++m) ra[m] = ard[m] + rso;                            \
    rb[0] = brd[0] + rso; rb[1] = brd[1] + rso;                                                    \
    rso += G1_SLOT; if (rso == G1_R * G1_SLOT) rso = 0;                                            \
    asm volatile("" : "+s"(rso));                                                                  \
  } while (0)
#define G1_MM(Z, st, i)                                                                            \
  do {                                                                                             \
    const int m_ = (i) / 7, n_ = (i) % 7;                                                          \
    u32x4 a4 = {fa[st][m_][0][0], fa[st][m_][0][1], fa[st][m_][1][0], fa[st][m_][1][1]};           \
    u32x4 b4 = {fb[st][n_][0][0], fb[st][n_][0][1], fb[st][n_][1][0], fb[st][n_][1][1]};           \
    if (Z) asm volatile("v_mfma_f32_16x16x32_bf16 %0, %1, %2, 0" : "=&v"(acc[n_][m_]) : "v"(b4), "v"(a4)); \
    else asm volatile("v_mfma_f32_16x16x32_bf16 %0, %1, %2, %0" : "+v"(acc[n_][m_]) : "v"(b4), "v"(a4)); \
  } while (0)
#define G1_LD(TR, st, STG)                                                                         \
  do {                                                                                             \
    _Pragma("unroll") for (int i = 0; i < 22; ++i) G1_RDX(TR, 1 - (st), i);                        \
    if (STG) { G1_GB(sb1, 2048); G1_GB(sb3, 3072); G1_GA(0); G1_GA(1); G1_ADV; }                   \
    G1_RADDR;                                                                                      \
    SCHED;                                                                                         \
  } while (0)
#define G1_IT(MODE, st, VM, STG, HOOK, EXTRA)                                                      \
  do {                                                                                             \
    SCHED;                                                                                         \
    asm volatile("s_waitcnt vmcnt(" #VM ") lgkmcnt(0)\n\ts_barrier" ::: "memory");                 \
    SCHED;                                                                                         \
    EXTRA;                                                                                         \
    if (grp) G1_LD(TRRD, st, STG);                                                                 \
    SCHED;                                                                                         \
    _Pragma("unroll") for (int i = 0; i < 28; ++i) {                                               \
      G1_MM(MODE, st, i);                                                                            \
      SCHED;                                                                                       \
      HOOK;                                                                                        \
      SCHED;                                                                                       \
    }                                                                                              \
    if (!grp) G1_LD(TRRD2, st, STG);                                                               \
    SCHED;                                                                                         \
  } while (0)

  G1_RADDR;
  SCHED;
  asm volatile("s_waitcnt vmcnt(19)\n\ts_barrier" ::: "memory");
  SCHED;
#pragma unroll
  for (int i = 0; i < 22; ++i) G1_RD(0, i);
  G1_RADDR;
  SCHED;
  TS(3, 1);
  TSC(5, 0);
  unsigned long long sg[4];
  int tid2;
  gu64* sp;
#define G1_MAIN(MODE)                                                                              \
  do {                                                                                             \
    G1_IT(1, 0, 15, 1, {}, {});                                                                    \
    G1_IT(MODE, 1, 15, 1, {}, {});                                                                 \
    G1_IT(MODE, 0, 15, 1, {}, {});                                                                 \
    G1_IT(MODE, 1, 15, 1, {}, {});                                                                 \
    float smm, sqq, sm[8], sq[8];                                                                  \
    G1_IT(MODE, 0, 12, 1, {                                                                        \
      if (i == 0) { asm volatile("" : "+v"(xa), "+v"(xb), "+v"(m1v) : : "memory"); }               \
      if (i < 8) wf_merge(sm[i], sq[i], (i & 1) ? bfhi(xa[i >> 1]) : bflo(xa[i >> 1]), 0.f,        \
                          (i & 1) ? bfhi(xb[i >> 1]) : bflo(xb[i >> 1]), 0.f, 0.5f);               \
      else if (i < 12)                                                                             \
        wf_swap32(sm[i & 3], sq[i & 3], sm[i & 3], sq[i & 3], sm[(i & 3) + 4], sq[(i & 3) + 4], 1.f); \
      else if (i < 14)                                                                             \
        wf_swap16(sm[i & 1], sq[i & 1], sm[i & 1], sq[i & 1], sm[(i & 1) + 2], sq[(i & 1) + 2], 2.f); \
      else if (i == 14) {                                                                          \
        const bool hi8 = lane & 8;                                                                 \
        float mo = dppz<0x128>(hi8 ? sm[0] : sm[1]);                                               \
        float qo = dppz<0x128>(hi8 ? sq[0] : sq[1]);                                               \
        wf_merge(smm, sqq, hi8 ? sm[1] : sm[0], hi8 ? sq[1] : sq[0], mo, qo, 4.f);                 \
      } else if (i == 15) wf_dpp<0xB1>(smm, sqq, 8.f);                                             \
      else if (i == 16) wf_dpp<0x4E>(smm, sqq, 16.f);                                              \
      else if (i == 17) wf_dpp<0x141>(smm, sqq, 32.f);                                             \
      else if (i == 18) {                                                                          \
               \
        u32x2 v;                                                                                   \
        v[0] = __float_as_uint(smm);                                                               \
        v[1] = __float_as_uint(sqq);                                                               \
        u32 ad = lds0 + w * 4096 + 2048 + ((w & 1) ? 0 : 896) + lane;                              \
        asm volatile("s_mov_b64 exec, %2\n\tds_write_b64 %0, %1\n\ts_mov_b64 exec, -1"             \
                     ::"v"(ad), "v"(v), "s"(0x0101010101010101ull) : "memory");                    \
      }                                                                                            \
    }, {});                                                                                        \
    G1_IT(MODE, 1, 12, 1, {}, {                                                                    \
      if (w == 0) {                                                                                \
        u32x2 v;                                                                                   \
        int l1 = lane;                                                                             \
        asm volatile("" : "+v"(l1));                                                               \
        u32 ad = lds0 + (l1 >> 3) * 4096 + 2048 + ((l1 & 8) ? 0 : 896) + (l1 & 7) * 8;             \
        asm volatile("ds_read_b64 %0, %1\n\ts_waitcnt lgkmcnt(0)" : "=v"(v) : "v"(ad) : "memory"); \
        float mm = __uint_as_float(v[0]);                                                          \
        float qq = __uint_as_float(v[1]);                                                          \
        wf_dpp<0x128>(mm, qq, 64.f);                                                               \
        wf_swap16(mm, qq, mm, qq, mm, qq, 128.f);                                                  \
        wf_swap32(mm, qq, mm, qq, mm, qq, 256.f);                                                  \
        float r2 = rsqrtf(qq * (1.0f / D_) + 1e-5f);                                               \
        float rd = r2 * (mm - m1v);                                                                \
        u32 rb_ = __float_as_uint(r2);                                                             \
        if (rb_ == 0u) rb_ = 0x80000000u;                                                          \
        if (l1 < 8)                                                                                \
          __hip_atomic_store((gu64*)(st2 + r0 + l1),                                               \
                             ((unsigned long long)__float_as_uint(rd) << 32) | rb_,                \
                             __ATOMIC_RELAXED, __HIP_MEMORY_SCOPE_AGENT);                          \
      }                                                                                            \
      SCHED;                                                                                       \
    });                                                                                            \
    u32 pfd;                                                                                       \
    {                                                                                              \
            \
      const u32 po = (tid < 448 ? tid : 447) * 64;                                                 \
      asm volatile("global_load_dword %0, %1, %2"                                                  \
                   : "=v"(pfd) : "v"(po), "s"((const char*)w2 + (size_t)b * 28672) : "memory");    \
    }                                                                                              \
    TSC(2, 0);                                                                                     \
    const char* xf = xch + (size_t)b * 20480;                                                      \
    const u32 xfo = tid * 4;                                                                       \
    const u32 ones = 0xFFFFFFFFu;                                                                  \
    for (int t = 6; t < 26; t += 2) {                                                              \
      G1_IT(MODE, 0, 12, 1, {}, {                                  \
        asm volatile("global_store_dword %0, %1, %2 " XFL ::"v"(xfo), "v"(ones), "s"(xf) : "memory"); \
        xf += 2048;                                                                                \
        SCHED;                                                                                     \
      });                                                                                          \
      G1_IT(MODE, 1, 12, 1, {}, {});                                                               \
    }                                                                                              \
    TSC(2, 1);                                                                                     \
    asm volatile("" ::"v"(pfd));                                                                   \
    tid2 = tid;                                                                                    \
    asm volatile("" : "+v"(tid2));                                                                 \
    float cp[8];                                                                                   \
    G1_IT(MODE, 0, 12, 1, {}, {                                      \
      const u32 co = (tid2 < 448 ? tid2 : 447) * 4;                                                \
      _Pragma("unroll") for (int ks = 0; ks < 8; ++ks)                                             \
        asm volatile("global_load_dword %0, %1, %2"                                                \
                     : "=v"(cp[ks]) : "v"(co), "s"(c12 + (ks * 32 + nt) * 448) : "memory");        \
      SCHED;                                                                                       \
    });                                                                                            \
    sp = (gu64*)(st2 + s0 + wm * 64 + (tid2 & 15));                                                \
    G1_IT(MODE, 1, 20, 0, {}, {                                                                    \
      _Pragma("unroll") for (int m = 0; m < 4; ++m)                                                \
        asm volatile("global_load_dwordx2 %0, %1, off offset:%2 sc1"                               \
                     : "=v"(sg[m]) : "v"(sp), "i"(128 * m) : "memory");                            \
      SCHED;                                                                                       \
    });                                                                                            \
    G1_IT(MODE, 0, 20, 0, {}, {});                                                                 \
    G1_IT(MODE, 1, 8, 0, {}, {                       \
      asm volatile("" : "+v"(cp[0]), "+v"(cp[1]), "+v"(cp[2]), "+v"(cp[3]), "+v"(cp[4]),           \
                        "+v"(cp[5]), "+v"(cp[6]), "+v"(cp[7]) : : "memory");                       \
      float csum = ((cp[0] + cp[1]) + (cp[2] + cp[3])) + ((cp[4] + cp[5]) + (cp[6] + cp[7]));      \
      u32 ad = lds0 + 2 * G1_SLOT + tid2 * 4;                                                      \
      asm volatile("ds_write_b32 %0, %1" ::"v"(ad), "v"(csum) : "memory");                         \
      SCHED;                                                                                       \
    });                                                                                            \
    G1_IT(MODE, 0, 4, 0, {}, {});                                                                  \
    asm volatile("s_waitcnt vmcnt(0) lgkmcnt(0)"                                                   \
                 : "+v"(sg[0]), "+v"(sg[1]), "+v"(sg[2]), "+v"(sg[3]) : : "memory");               \
    SCHED;                                                                                         \
    _Pragma("unroll") for (int i = 0; i < 28; ++i) G1_MM(0, 1, i);                                   \
    asm volatile("s_nop 7\n\ts_nop 7" ::: "memory");                                               \
    SCHED;                                                                                         \
  } while (0)
  G1_MAIN(0);
  const int lane2 = tid2 & 63, g = lane2 >> 4;
  TS(3, 2);
  TSC(5, 1);

  {
    for (int spin = 0; spin < (1 << 22); ++spin) {
      bool ok = ((u32)sg[0] != 0u) & ((u32)sg[1] != 0u) & ((u32)sg[2] != 0u) & ((u32)sg[3] != 0u);
      if (__all(ok)) break;
      __builtin_amdgcn_s_sleep(8);
#pragma unroll
      for (int m = 0; m < 4; ++m) sg[m] = __hip_atomic_load(sp + 16 * m, __ATOMIC_RELAXED, __HIP_MEMORY_SCOPE_AGENT);
    }
    float r2[4], rd[4];
#pragma unroll
    for (int m = 0; m < 4; ++m) {
      r2[m] = __uint_as_float((u32)sg[m]);
      rd[m] = -__uint_as_float((u32)(sg[m] >> 32));
    }
    const float* cl = (const float*)(smem + 2 * G1_SLOT) + wn * 56;
    char* ob = (char*)hh + ((size_t)((s0 + wm * 64) >> 4) * 448 + nt * 14 + wn * 7) * 256 + (lane2 & 15) * 16;
#pragma unroll
    for (int j = 0; j < 3; ++j) {
      const f32x4 c1g = *(const f32x4*)(cl + 16 * j + 4 * g), c1u = *(const f32x4*)(cl + 112 + 16 * j + 4 * g);
      const f32x4 c2g = *(const f32x4*)(cl + 224 + 16 * j + 4 * g), c2u = *(const f32x4*)(cl + 336 + 16 * j + 4 * g);
      u32 o[4][2];
#pragma unroll
      for (int m = 0; m < 4; ++m) {
        float hv[4];
#pragma unroll
        for (int r = 0; r < 4; ++r) {
          float gt = fmaf(acc[2 * j][m][r], r2[m], fmaf(rd[m], c1g[r], c2g[r]));
          float up = fmaf(acc[2 * j + 1][m][r], r2[m], fmaf(rd[m], c1u[r], c2u[r]));
          hv[r] = silu_f(gt) * up;
        }
        o[m][0] = pk2(hv[0], hv[1]);
        o[m][1] = pk2(hv[2], hv[3]);
      }
#pragma unroll
      for (int mp = 0; mp < 2; ++mp) {
        auto d0 = __builtin_amdgcn_permlane16_swap(o[2 * mp][0], o[2 * mp + 1][0], false, false);
        auto d1 = __builtin_amdgcn_permlane16_swap(o[2 * mp][1], o[2 * mp + 1][1], false, false);
        u32x4 ov = {d0[0], d1[0], d0[1], d1[1]};
        ST16(ob + ((size_t)(2 * mp + (g & 1)) * 448 + 2 * j + (g >> 1)) * 256, ov, STPG);
      }
    }
    {
      const int co = (g >> 1) * 112 + 48 + 4 * (g & 1);
      const f32x4 c1 = *(const f32x4*)(cl + co), c2 = *(const f32x4*)(cl + 224 + co);
      u32 o[2][2];
#pragma unroll
      for (int mm = 0; mm < 2; ++mm) {
        float hv[4];
#pragma unroll
        for (int r = 0; r < 4; ++r) {
          float va = fmaf(acc[6][mm][r], r2[mm], fmaf(rd[mm], c1[r], c2[r]));
          float vb = fmaf(acc[6][mm + 2][r], r2[mm + 2], fmaf(rd[mm + 2], c1[r], c2[r]));
          auto sw = __builtin_amdgcn_permlane32_swap(__float_as_uint(va), __float_as_uint(vb), false, false);
          hv[r] = silu_f(__uint_as_float(sw[0])) * __uint_as_float(sw[1]);
        }
        o[mm][0] = pk2(hv[0], hv[1]);
        o[mm][1] = pk2(hv[2], hv[3]);
      }
      auto d0 = __builtin_amdgcn_permlane16_swap(o[0][0], o[1][0], false, false);
      auto d1 = __builtin_amdgcn_permlane16_swap(o[0][1], o[1][1], false, false);
      u32x4 ov = {d0[0], d1[0], d0[1], d1[1]};
      ST16(ob + ((size_t)g * 448 + 6) * 256, ov, STPG);
    }
  }
  TS(3, 3);
}

#define G2_BSLOT 8192
#define G2_XOFF 98304
#define G2_LDS (G2_XOFF + 65536)

__global__ __launch_bounds__(256, 1) void g2_gemm(const u16* __restrict__ hh, const u16* __restrict__ w2,
                                                  const u16* __restrict__ x1T, const float* __restrict__ modf,
                                                  char* xch, u16* __restrict__ out) {
  extern __shared__ __attribute__((aligned(16))) char smem[];
  const int tid = threadIdx.x, lane = tid & 63;
  const int w = __builtin_amdgcn_readfirstlane(tid >> 6);
  const int b = blockIdx.x;
  const int xcd = b & 7, idx = b >> 3;
  const int kh = xcd & 1;
  const int mt = (xcd >> 1) * 4 + (idx & 3), nt = idx >> 2;
  const int s0 = mt * 128, d0 = nt * 128;
  const u32 lds0 = (u32)(uintptr_t)(__attribute__((address_space(3))) char*)smem;
  TS(4, 0);

  u32 boff[8];
#pragma unroll
  for (int i = 0; i < 8; ++i) {
    const int f = 8 * (i & 3) + 4 * (i >> 2) + (lane >> 4);
    const int c = (lane & 15) ^ ((4 * (i & 1) + (lane >> 4)) << 1);
    boff[i] = (u32)(f * (D_ * 2) + c * 16 + 3072 - (i & 3) * 1024);
  }
  const char* bbase = (const char*)w2 + (size_t)(1792 * kh + 32 * w) * (D_ * 2) + d0 * 2 - 3072;
  const u32 bdst = lds0 + w * (3 * G2_BSLOT);
  const char* ab[8];
#pragma unroll
  for (int m = 0; m < 8; ++m)
    ab[m] = (const char*)hh + (size_t)((s0 >> 4) + 4 * ((m >> 2) ^ kh ^ 1) + ((m + w) & 3)) * (448 * 256);
  u32 aoff = kh * 57344 + w * 1024 + lane * 16;
#define G2_LDA(set, m) \
  asm volatile("global_load_dwordx4 %0, %1, %2" : "=v"(A[set][m]) : "v"(aoff), "s"(ab[m]) : "memory")
#define G2_LDB2(slot, i)                                                     \
  asm volatile("s_add_u32 m0, %0, %1\n\ts_nop 0\n\tglobal_load_lds_dwordx4 %2, %4 offset:%5\n\t"     \
               "global_load_lds_dwordx4 %3, %4 offset:%6"                                            \
               ::"s"(bdst), "i"((slot) * G2_BSLOT + ((i) >> 2) * 4096), "v"(boff[i]), "v"(boff[(i) + 1]), \
                 "s"(bbase), "i"(((i) & 3) * 1024), "i"(((i) & 3) * 1024 + 1024) : "memory", "scc")
#define G2_ADV do { aoff += 4096; bbase += (size_t)128 * D_ * 2; } while (0)

  f32x4 acc[8][8];
  u32x4 A[3][8];
  u32x2 bf[2][8][2];
#define G2_PRO(set)                                               \
  do {                                                            \
    _Pragma("unroll") for (int m = 0; m < 8; ++m) {               \
      G2_LDA(set, m);                                             \
      if (m < 4) G2_LDB2(set, 2 * m);                             \
    }                                                             \
    SCHED;                                                        \
    G2_ADV;                                                       \
  } while (0)
  G2_PRO(0); G2_PRO(1);
#pragma unroll
  for (int i = 0; i < 4; ++i)
    GLDS16((const char*)x1T + (size_t)(d0 + 32 * i + (lane >> 1)) * (S_ * 2) + (s0 + 64 * kh + 16 * w) * 2 + (lane & 1) * 16,
           smem + G2_XOFF + w * 16384 + i * 1024);
  SCHED;
  G2_PRO(2);

  const int g = lane >> 4, q = (lane >> 2) & 3, p = lane & 3;
  u32 brd[8];
  {
    const int row = 4 * g + q;
#pragma unroll
    for (int n = 0; n < 8; ++n) brd[n] = bdst + row * 256 + ((n ^ (row & 7)) * 32) + p * 8;
  }
#define G2_RDB(bs, slot, n)                                                \
  do {                                                                     \
    TRRD(bf[bs][n][0], brd[n], (slot) * G2_BSLOT);                         \
    TRRD(bf[bs][n][1], brd[n], (slot) * G2_BSLOT + 4096);                  \
  } while (0)
  WAIT_V(40);
  TS(2, 2);
#pragma unroll
  for (int n = 0; n < 8; ++n) G2_RDB(0, 0, n);
#define G2_B4(bs, n) ((u32x4){bf[bs][n][0][0], bf[bs][n][0][1], bf[bs][n][1][0], bf[bs][n][1][1]})
#define G2_BOPS(bs)                                                                                  \
  "v"(G2_B4(bs, 0)), "v"(G2_B4(bs, 1)), "v"(G2_B4(bs, 2)), "v"(G2_B4(bs, 3)), "v"(G2_B4(bs, 4)),     \
      "v"(G2_B4(bs, 5)), "v"(G2_B4(bs, 6)), "v"(G2_B4(bs, 7))
#define G2_ACC(C, m)                                                                                 \
  C(acc[0][m]), C(acc[1][m]), C(acc[2][m]), C(acc[3][m]), C(acc[4][m]), C(acc[5][m]), C(acc[6][m]), C(acc[7][m])
#define G2_MFMA8                                           \
  "v_mfma_f32_16x16x32_bf16 %0, %9, %8, %0\n\t"            \
  "v_mfma_f32_16x16x32_bf16 %1, %10, %8, %1\n\t"           \
  "v_mfma_f32_16x16x32_bf16 %2, %11, %8, %2\n\t"           \
  "v_mfma_f32_16x16x32_bf16 %3, %12, %8, %3\n\t"           \
  "v_mfma_f32_16x16x32_bf16 %4, %13, %8, %4\n\t"           \
  "v_mfma_f32_16x16x32_bf16 %5, %14, %8, %5\n\t"           \
  "v_mfma_f32_16x16x32_bf16 %6, %15, %8, %6\n\t"           \
  "v_mfma_f32_16x16x32_bf16 %7, %16, %8, %7\n\t"
#define G2_MFMA8Z                                          \
  "v_mfma_f32_16x16x32_bf16 %0, %9, %8, 0\n\t"             \
  "v_mfma_f32_16x16x32_bf16 %1, %10, %8, 0\n\t"            \
  "v_mfma_f32_16x16x32_bf16 %2, %11, %8, 0\n\t"            \
  "v_mfma_f32_16x16x32_bf16 %3, %12, %8, 0\n\t"            \
  "v_mfma_f32_16x16x32_bf16 %4, %13, %8, 0\n\t"            \
  "v_mfma_f32_16x16x32_bf16 %5, %14, %8, 0\n\t"            \
  "v_mfma_f32_16x16x32_bf16 %6, %15, %8, 0\n\t"            \
  "v_mfma_f32_16x16x32_bf16 %7, %16, %8, 0\n\t"
#define G2_MML(set, bs, m, WV)                                                                       \
  asm volatile("s_waitcnt vmcnt(%19)\n\t" G2_MFMA8 "global_load_dwordx4 %8, %17, %18"                \
               : G2_ACC("+a", m), "+v"(A[set][m])                                                    \
               : G2_BOPS(bs), "v"(aoff), "s"(ab[m]), "i"(WV) : "memory")
#define G2_MML0(set, bs, m, WV)                                                                      \
  asm volatile("s_waitcnt vmcnt(%19)\n\t" G2_MFMA8Z "global_load_dwordx4 %8, %17, %18"               \
               : G2_ACC("=a", m), "+v"(A[set][m])                                                    \
               : G2_BOPS(bs), "v"(aoff), "s"(ab[m]), "i"(WV) : "memory")
#define G2_MM(set, bs, m, WV)                                                                        \
  asm volatile("s_waitcnt vmcnt(%17)\n\t" G2_MFMA8                                                   \
               : G2_ACC("+a", m) : "v"(A[set][m]), G2_BOPS(bs), "i"(WV) : "memory")
#define G2_RDN(set, bs, m)                                                                    \
  do {                                                                                        \
    if ((m) >= 4 && (m) < 7) {                                                                \
      G2_RDB((bs) ^ 1, ((set) + 1) % 3, 3 * ((m) - 4));                                       \
      G2_RDB((bs) ^ 1, ((set) + 1) % 3, 3 * ((m) - 4) + 1);                                   \
      if ((m) < 6) G2_RDB((bs) ^ 1, ((set) + 1) % 3, (3 * ((m) - 4) + 2) & 7);                \
    }                                                                                         \
  } while (0)
#define G2_STEP(set, bs, MML)                                                                 \
  do {                                                                                        \
    WAIT_L(0);                                                                                \
    SCHED;                                                                                    \
    _Pragma("unroll") for (int m = 0; m < 8; ++m) {                                           \
      MML(set, bs, m, 47);                                                                    \
      if (m < 4) G2_LDB2(set, 2 * (m & 3));                                                   \
      if (m == 4) WAIT_V(33);                                                                 \
      G2_RDN(set, bs, m);                                                                     \
      SCHED;                                                                                  \
    }                                                                                         \
    G2_ADV;                                                                                   \
  } while (0)
#define G2_TAIL(set, bs, NEW, NEXT)                                                           \
  do {                                                                                        \
    WAIT_L(0);                                                                                \
    SCHED;                                                                                    \
    _Pragma("unroll") for (int m = 0; m < 8; ++m) {                                           \
      G2_MM(set, bs, m, (NEW) + 15 - (m < 4 ? 3 * m : 8 + m));                                \
      if (NEXT) {                                                                             \
        if (m == 4) asm volatile("s_waitcnt vmcnt(%0)" ::"i"((NEXT) ? (NEW) - 12 : 0) : "memory"); \
        G2_RDN(set, bs, m);                                                                   \
      }                                                                                       \
      SCHED;                                                                                  \
    }                                                                                         \
  } while (0)
  G2_STEP(0, 0, G2_MML0);
  TS(2, 3);
  G2_STEP(1, 1, G2_MML);
  G2_STEP(2, 0, G2_MML);
  TS(2, 4);
  G2_STEP(0, 1, G2_MML);
  G2_STEP(1, 0, G2_MML);
  G2_STEP(2, 1, G2_MML);
  G2_STEP(0, 0, G2_MML);
  G2_STEP(1, 1, G2_MML);
  G2_STEP(2, 0, G2_MML);
  G2_STEP(0, 1, G2_MML);
  G2_STEP(1, 0, G2_MML);
  TS(2, 5);
  u32x2 xu[8];
  {
    const u32 xb = lds0 + G2_XOFF + w * 16384 + (4 * g + q) * 32 + p * 8;
#pragma unroll
    for (int n = 0; n < 8; ++n) TRRD(xu[n], xb, n * 512);
  }
  G2_TAIL(2, 1, 32, true);
  const int tile = mt * 8 + nt;
  const char* xin = xch + (size_t)((tile * 2 + kh) * 4 + w) * 4096;
  const char* xout = xch + (size_t)((tile * 2 + (kh ^ 1)) * 4 + w) * 4096;
  const u32 xo = lane * 16;
  const u32 rwa = bdst + xo, rwe = lds0 + G2_XOFF + w * 16384 + xo;
  u32 rra[3];
#pragma unroll
  for (int j = 0; j < 3; ++j) rra[j] = lds0 + ((w - 1 - j) & 3) * (3 * G2_BSLOT) + j * 8192 + xo;
  u32x4 pv[4];
  f32x4 tr[8][3], va;
#define G2_LDP                                                                                       \
  _Pragma("unroll") for (int c = 0; c < 4; ++c)                                                      \
    asm volatile("global_load_dwordx4 %0, %1, %2 offset:%3 " XLD                                     \
                 : "=v"(pv[c]) : "v"(xo), "s"(xin), "i"(c * 1024) : "memory")
#define G2_RW(ad, m, n)                                                                              \
  asm volatile("ds_write_b128 %0, %1 offset:%2"                                                      \
               ::"v"(ad), "a"(acc[n][m]), "i"((((m) & 3) - 1) * 8192 + (n) * 1024) : "memory")
#define G2_RRD                                                                                       \
  _Pragma("unroll") for (int n = 0; n < 8; ++n) _Pragma("unroll") for (int j = 0; j < 3; ++j)        \
    asm volatile("ds_read_b128 %0, %1 offset:%2" : "=v"(tr[n][j]) : "v"(rra[j]), "i"(n * 1024) : "memory")
#define G2_RWT(n, c)                                                                                 \
  asm volatile("s_waitcnt lgkmcnt(" #c ")"                                                           \
               : "+v"(tr[n][0]), "+v"(tr[n][1]), "+v"(tr[n][2]), "+v"(tr[n + 1][0]), "+v"(tr[n + 1][1]), \
                 "+v"(tr[n + 1][2]), "+v"(tr[n + 2][0]), "+v"(tr[n + 2][1]), "+v"(tr[n + 2][2]),     \
                 "+v"(tr[n + 3][0]), "+v"(tr[n + 3][1]), "+v"(tr[n + 3][2]) : : "memory")
#define G2_SEND(n)                                                                                   \
  do {                                                                                               \
    const f32x4 vb = (acc[n][0] + tr[n][0]) + (tr[n][1] + tr[n][2]);                                 \
    if ((n) & 1) {                                                                                   \
      u32x4 u = {pk2(va[0], va[1]), pk2(va[2], va[3]), pk2(vb[0], vb[1]), pk2(vb[2], vb[3])};        \
      _Pragma("unroll") for (int r = 0; r < 4; ++r)                   \
        u[r] = u[r] < 0xFFFFFFFEu ? u[r] : 0xFFFFFFFEu;                                              \
      asm volatile("global_store_dwordx4 %0, %1, %2 offset:%3 " XST "\n\ts_nop 1"                    \
                   ::"v"(xo), "v"(u), "s"(xout), "i"(((n) >> 1) * 1024) : "memory");                 \
    } else {                                                                                         \
      va = vb;                                                                                       \
      asm volatile("" : "+v"(va));                                                                   \
    }                                                                                                \
  } while (0)
#define G2_G(st, m, FILL)                                                                            \
  _Pragma("unroll") for (int i = 0; i < 4; ++i) {                                                    \
    asm volatile("v_mfma_f32_16x16x32_bf16 %0, %3, %2, %0\n\tv_mfma_f32_16x16x32_bf16 %1, %4, %2, %1" \
                 : "+a"(acc[2 * i][m]), "+a"(acc[2 * i + 1][m])                                      \
                 : "v"(A[st][m]), "v"(G2_B4(st, 2 * i)), "v"(G2_B4(st, 2 * i + 1)));                 \
    SCHED;                                                                                           \
    FILL;                                                                                            \
    SCHED;                                                                                           \
  }
  asm volatile("s_waitcnt vmcnt(16) lgkmcnt(0)"
               : "+v"(xu[0]), "+v"(xu[1]), "+v"(xu[2]), "+v"(xu[3]), "+v"(xu[4]), "+v"(xu[5]), "+v"(xu[6]),
                 "+v"(xu[7]) : : "memory");
  SCHED;
  G2_G(0, 1, {});
  G2_G(0, 2, {});
  WAIT_V(4);
  G2_G(0, 3, { G2_RDB(1, 1, 2 * i); G2_RDB(1, 1, 2 * i + 1); });
  G2_G(0, 0, {});
  WAIT_L(0);
  SCHED;
  G2_G(1, 1, {});
  G2_G(1, 2, G2_RW(rwa, 1, i));
  G2_G(1, 3, G2_RW(rwa, 1, 4 + i));
  G2_G(1, 0, G2_RW(rwa, 2, i));
  G2_G(0, 5, G2_RW(rwa, 2, 4 + i));
  WAIT_V(0);
  G2_G(1, 5, G2_RW(rwa, 3, i));
  G2_G(0, 6, G2_RW(rwa, 3, 4 + i));
  TS(4, 1);
  SYNC_L;
  TS(4, 7);
  G2_RRD;
  SCHED;
  asm volatile("" : G2_ACC("+a", 0) : : "memory");
  G2_G(1, 6, {});
  G2_RWT(0, 12);
  G2_G(0, 7, { G2_SEND(i); if (i & 1) { G2_RW(rwe, 5, i - 1); G2_RW(rwe, 5, i); } });
  G2_RWT(4, 4);
  G2_G(1, 7, { G2_SEND(4 + i); G2_RW(rwe, 5, 4 + i); });
  TS(4, 2);
  BAR;
  TS(2, 6);
  G2_G(0, 4, { G2_RW(rwe, 6, 2 * i); G2_RW(rwe, 6, 2 * i + 1); });
  G2_G(1, 4, { G2_RW(rwa, 7, 2 * i); G2_RW(rwa, 7, 2 * i + 1); });
  G2_LDP;
  f32x4 gbv[8];
#pragma unroll
  for (int n = 0; n < 8; ++n) gbv[n] = *(const f32x4*)(modf + d0 + 16 * n + 4 * g);
#pragma unroll
  for (int j = 0; j < 2; ++j) rra[j] = lds0 + G2_XOFF + ((w - 1 - j) & 3) * 16384 + j * 8192 + xo;
  SCHED;
  TS(2, 7);
  asm volatile("s_nop 7\n\ts_nop 7\n\ts_waitcnt lgkmcnt(0)\n\ts_barrier" : G2_ACC("+a", 4) : : "memory");
  TS(4, 6);
  G2_RRD;
  f32x4 fin[8];
#pragma unroll
  for (int n = 0; n < 8; ++n) {
    if (n == 0) G2_RWT(0, 12);
    if (n == 4) G2_RWT(4, 0);
    fin[n] = (acc[n][4] + tr[n][0]) + (tr[n][1] + tr[n][2]);
    asm volatile("" : "+v"(fin[n]));
  }
  SCHED;
  TS(4, 3);
  for (int spin = 0; spin < (1 << 22); ++spin) {
    asm volatile("s_waitcnt vmcnt(0)"
                 : "+v"(pv[0]), "+v"(pv[1]), "+v"(pv[2]), "+v"(pv[3]) : : "memory");
    u32 mx = 0;
#pragma unroll
    for (int n = 0; n < 4; ++n)
#pragma unroll
      for (int r = 0; r < 4; ++r) mx = mx > pv[n][r] ? mx : pv[n][r];
    if (__all(mx != 0xFFFFFFFFu)) break;
    __builtin_amdgcn_s_sleep(G2_SLEEP);
    G2_LDP;
  }
  TS(4, 4);
  {
    char* ob = (char*)out + (size_t)(s0 + 64 * kh + 16 * w + (lane & 15)) * (D_ * 2) + (d0 + 16 * (g & 1) + 8 * (g >> 1)) * 2;
#pragma unroll
    for (int np = 0; np < 4; ++np) {
      u32 o[2][2];
#pragma unroll
      for (int mm = 0; mm < 2; ++mm) {
        const int n = 2 * np + mm;
        const f32x4 v = fin[n] + (f32x4){bflo(pv[np][2 * mm]), bfhi(pv[np][2 * mm]), bflo(pv[np][2 * mm + 1]),
                                         bfhi(pv[np][2 * mm + 1])};
        o[mm][0] = pk2(fmaf(gbv[n][0], v[0], bflo(xu[n][0])), fmaf(gbv[n][1], v[1], bfhi(xu[n][0])));
        o[mm][1] = pk2(fmaf(gbv[n][2], v[2], bflo(xu[n][1])), fmaf(gbv[n][3], v[3], bfhi(xu[n][1])));
      }
      auto e0 = __builtin_amdgcn_permlane16_swap(o[0][0], o[1][0], false, false);
      auto e1 = __builtin_amdgcn_permlane16_swap(o[0][1], o[1][1], false, false);
      u32x4 ov = {e0[0], e1[0], e0[1], e1[1]};
      *(u32x4*)(ob + np * 64) = ov;
    }
  }
  TS(4, 5);
}

extern "C" void kernel_launch(void* const* d_in, const int* in_sizes, int n_in, void* d_out, int out_size,
                              void* d_ws, size_t ws_size, hipStream_t stream) {
  const u16* x = (const u16*)d_in[0];
  const u16* k = (const u16*)d_in[1];
  const u16* v = (const u16*)d_in[2];
  const u16* shk = (const u16*)d_in[3];
  const u16* ssmk = (const u16*)d_in[4];
  const u16* ssmD = (const u16*)d_in[5];
  const u16* w1 = (const u16*)d_in[7];
  const u16* w3 = (const u16*)d_in[8];
  const u16* w2 = (const u16*)d_in[9];
  const u16* c = (const u16*)d_in[10];
  const u16* wmod = (const u16*)d_in[11];
  char* ws = (char*)d_ws;
  u16* kvT = (u16*)(ws + WS_KVT);
  u16* xT = (u16*)(ws + WS_XT);
  u16* x1T = (u16*)(ws + WS_X1T);
  u16* apT = (u16*)(ws + WS_APT);
  u16* hh = (u16*)(ws + WS_HHT);
  float* modp = (float*)(ws + WS_MODP);
  f32x2* st1 = (f32x2*)(ws + WS_ST1);
  float* modf = (float*)(ws + WS_MOD);
  f32x2* st2 = (f32x2*)(ws + WS_ST2);
  float* c12 = (float*)(ws + WS_C12);

  (void)hipFuncSetAttribute((const void*)k1_conv, hipFuncAttributeMaxDynamicSharedMemorySize, K1_LDS);
  (void)hipFuncSetAttribute((const void*)g1_gemm, hipFuncAttributeMaxDynamicSharedMemorySize, G1_LDS);
  (void)hipFuncSetAttribute((const void*)g2_gemm, hipFuncAttributeMaxDynamicSharedMemorySize, G2_LDS);

  k0_prep<<<K0_TRB + K0_MODB + K0_STB, 256, 0, stream>>>(x, k, v, shk, c, wmod, kvT, xT, st1, modp, st2);
  k1_conv<<<256, 512, K1_LDS, stream>>>(ssmk, ssmD, kvT, xT, st1, modp, w1, w3, x1T, apT, modf, c12);
  g1_gemm<<<256, 512, G1_LDS, stream>>>(apT, w1, w3, x1T, st1, (unsigned long long*)st2, c12, w2, ws + WS_XCH, hh);
  g2_gemm<<<256, 256, G2_LDS, stream>>>(hh, w2, x1T, modf, ws + WS_XCH, (u16*)d_out);
}
```

```cpp
#include <hip/hip_runtime.h>
#include <hip/hip_bf16.h>
#include <stdint.h>


typedef unsigned short u16;
typedef unsigned int u32;
typedef __attribute__((ext_vector_type(8))) __bf16 bf16x8;
typedef __attribute__((ext_vector_type(2))) __bf16 bf16x2;
typedef __attribute__((ext_vector_type(4))) short s16x4;
typedef __attribute__((ext_vector_type(8))) short s16x8;
typedef __attribute__((ext_vector_type(4))) float f32x4;
typedef __attribute__((ext_vector_type(2))) float f32x2;
typedef __attribute__((ext_vector_type(4))) u32 u32x4;
typedef __attribute__((ext_vector_type(2))) u32 u32x2;

#define S_ 2048
#define D_ 1024
#define F_ 3584

#define WS_KVT (0u)
#define WS_XT (4u << 20)
#define WS_X1T (8u << 20)
#define WS_APT (12u << 20)
#define WS_HHT (16u << 20)
#define WS_MODP (32u << 20)
#define WS_ST1 (33u << 20)
#define WS_MOD ((33u << 20) + (64u << 10))
#define WS_ST2 ((33u << 20) + (128u << 10))
#define WS_C12 ((33u << 20) + (192u << 10))
#define WS_XCH (40u << 20)
#define XST "sc1"
#define XLD "sc1"
#define XFL "sc1"
#define G2_SLEEP 8

__device__ __forceinline__ float bflo(u32 u) { return __uint_as_float(u << 16); }
__device__ __forceinline__ float bfhi(u32 u) { return __uint_as_float(u & 0xffff0000u); }
__device__ __forceinline__ float bf1(u16 h) { return __uint_as_float(((u32)h) << 16); }
__device__ __forceinline__ u32 pk2(float a, float b) {
  bf16x2 r = {(__bf16)a, (__bf16)b};
  return __builtin_bit_cast(u32, r);
}
__device__ __forceinline__ float rbf(float a) {
  return __uint_as_float(((u32)__builtin_bit_cast(u16, (__bf16)a)) << 16);
}
__device__ __forceinline__ float silu_f(float g) { return g * __builtin_amdgcn_rcpf(1.0f + __expf(-g)); }

#define GLDS16(gptr, ldsptr)                                                        \
  __builtin_amdgcn_global_load_lds((const __attribute__((address_space(1))) u32*)(gptr), \
                                   (__attribute__((address_space(3))) u32*)(ldsptr), 16, 0, 0)
#define WAIT_V(n) asm volatile("s_waitcnt vmcnt(" #n ")" ::: "memory")
#define WAIT_L(n) asm volatile("s_waitcnt lgkmcnt(" #n ")" ::: "memory")
#define BAR __builtin_amdgcn_s_barrier()
#define SCHED __builtin_amdgcn_sched_barrier(0)
#define STP0 ""
#define STP1 "sc1"
#define STPG "sc1"
#define ST16(ptr, val, POL) asm volatile("global_store_dwordx4 %0, %1, off " POL "\n\ts_nop 1" ::"v"(ptr), "v"(val) : "memory")
#define ST8(ptr, val, POL) asm volatile("global_store_dwordx2 %0, %1, off " POL ::"v"(ptr), "v"(val) : "memory")


__device__ __forceinline__ float wave_sum(float v) {
#pragma unroll
  for (int o = 32; o > 0; o >>= 1) v += __shfl_xor(v, o, 64);
  return v;
}

#define K0_MODB 384
#define K0_STB 256
#define K0_TRB 256
__global__ __launch_bounds__(256) void k0_prep(const u16* __restrict__ x, const u16* __restrict__ kk,
                                               const u16* __restrict__ vv, const u16* __restrict__ shk,
                                               const u16* __restrict__ cvec, const u16* __restrict__ wmod,
                                               u16* __restrict__ kvT, u16* __restrict__ xT,
                                               f32x2* __restrict__ st1, float* __restrict__ modp,
                                               f32x2* __restrict__ st2) {
  __shared__ float red[8 * 256];
  const int tid = threadIdx.x;
  int b = blockIdx.x;
  if (b < K0_TRB) {
    const int dt = b & 15, stile = b >> 4;
    const int lane = tid & 63, w = tid >> 6;
    const int c8 = lane & 7, sq = lane >> 3;
    const int s0 = stile * 128 + w * 32 + sq * 4;
    const int d0 = dt * 64 + c8 * 8;
    u32x4 kr[7], vr[4], xr[4], sw[4];
#pragma unroll
    for (int i = 0; i < 7; ++i) {
      int s = s0 - 3 + i;
      int sc = s < 0 ? 0 : s;
      kr[i] = *(const u32x4*)(kk + (size_t)sc * D_ + d0);
    }
#pragma unroll
    for (int i = 0; i < 4; ++i) vr[i] = *(const u32x4*)(vv + (size_t)(s0 + i) * D_ + d0);
#pragma unroll
    for (int i = 0; i < 4; ++i) xr[i] = *(const u32x4*)(x + (size_t)(s0 + i) * D_ + d0);
#pragma unroll
    for (int i = 0; i < 4; ++i) sw[i] = *(const u32x4*)(shk + (size_t)d0 * 4 + i * 8);
    if (s0 == 0) {
#pragma unroll
      for (int i = 0; i < 3; ++i) kr[i] = (u32x4){0, 0, 0, 0};
    }
#pragma unroll
    for (int j = 0; j < 8; ++j) {
      u32 wa = sw[j >> 1][(j & 1) * 2], wb = sw[j >> 1][(j & 1) * 2 + 1];
      float w0 = bflo(wa), w1 = bfhi(wa), w2 = bflo(wb), w3 = bfhi(wb);
      float kf[7];
#pragma unroll
      for (int i = 0; i < 7; ++i) kf[i] = (j & 1) ? bfhi(kr[i][j >> 1]) : bflo(kr[i][j >> 1]);
      float o[4];
#pragma unroll
      for (int i = 0; i < 4; ++i) {
        float ks = kf[i] * w3;
        ks = fmaf(kf[i + 1], w2, ks);
        ks = fmaf(kf[i + 2], w1, ks);
        ks = fmaf(kf[i + 3], w0, ks);
        float vf = (j & 1) ? bfhi(vr[i][j >> 1]) : bflo(vr[i][j >> 1]);
        o[i] = ks * vf;
      }
      u32x2 ov = {pk2(o[0], o[1]), pk2(o[2], o[3])};
      ST8(kvT + (size_t)(d0 + j) * S_ + s0, ov, STP0);
      const u32 sel = (j & 1) ? 0x07060302u : 0x05040100u;
      u32x2 xv = {__builtin_amdgcn_perm(xr[1][j >> 1], xr[0][j >> 1], sel),
                  __builtin_amdgcn_perm(xr[3][j >> 1], xr[2][j >> 1], sel)};
      ST8(xT + (size_t)(d0 + j) * S_ + s0, xv, STP0);
    }
    return;
  }
  b -= K0_TRB;
  if (b < K0_MODB) {
    const int ks = b / 24, strip = b % 24;
    const int cl = tid & 31, rg = tid >> 5;
    u32x4 wv[8];
    u16 cv[8];
#pragma unroll
    for (int i = 0; i < 8; ++i) {
      int row = ks * 64 + rg + 8 * i;
      wv[i] = *(const u32x4*)(wmod + (size_t)row * (6 * D_) + strip * 256 + cl * 8);
      cv[i] = cvec[row];
    }
    float acc[8];
#pragma unroll
    for (int j = 0; j < 8; ++j) acc[j] = 0.f;
#pragma unroll
    for (int i = 0; i < 8; ++i) {
      float cf = bf1(cv[i]);
      float sc = rbf(cf / (1.0f + __expf(-cf)));
#pragma unroll
      for (int j = 0; j < 4; ++j) {
        acc[2 * j] = fmaf(sc, bflo(wv[i][j]), acc[2 * j]);
        acc[2 * j + 1] = fmaf(sc, bfhi(wv[i][j]), acc[2 * j + 1]);
      }
    }
    *(f32x4*)&red[rg * 256 + cl * 8] = (f32x4){acc[0], acc[1], acc[2], acc[3]};
    *(f32x4*)&red[rg * 256 + cl * 8 + 4] = (f32x4){acc[4], acc[5], acc[6], acc[7]};
    __syncthreads();
    float s = 0.f;
#pragma unroll
    for (int i = 0; i < 8; ++i) s += red[i * 256 + tid];
    modp[ks * (6 * D_) + strip * 256 + tid] = s;
    return;
  }
  b -= K0_MODB;
  {
    const int lane = tid & 63, w = tid >> 6;
    const int r0 = b * 8 + w * 2;
    u32x4 a[2][2];
#pragma unroll
    for (int r = 0; r < 2; ++r)
#pragma unroll
      for (int h = 0; h < 2; ++h) a[r][h] = *(const u32x4*)(x + (size_t)(r0 + r) * D_ + h * 512 + lane * 8);
#pragma unroll
    for (int r = 0; r < 2; ++r) {
      float s = 0.f;
#pragma unroll
      for (int h = 0; h < 2; ++h)
#pragma unroll
        for (int j = 0; j < 4; ++j) s += bflo(a[r][h][j]) + bfhi(a[r][h][j]);
      s = wave_sum(s);
      float m = s * (1.0f / D_);
      float q = 0.f;
#pragma unroll
      for (int h = 0; h < 2; ++h)
#pragma unroll
        for (int j = 0; j < 4; ++j) {
          float d0 = bflo(a[r][h][j]) - m, d1 = bfhi(a[r][h][j]) - m;
          q = fmaf(d0, d0, q);
          q = fmaf(d1, d1, q);
        }
      q = wave_sum(q);
      float rs = rsqrtf(q * (1.0f / D_) + 1e-5f);
      if (lane == 0) {
        st1[r0 + r] = (f32x2){m, rs};
        st2[r0 + r] = (f32x2){0.f, 0.f};
      }
    }
  }
}

#define K1_KOFF 0
#define K1_UOFF 4224
#define K1_XOFF 8576
#define K1_YOFF 12672
#define K1_COFF 20864
#define K1_WSZ 21408
#define K1_STOFF (4 * K1_WSZ)
#define K1_SLOFF (K1_STOFF + 16384)
#define K1_MVOFF (K1_SLOFF + 57344)
#define K1_LDS (K1_MVOFF + 512)

typedef __attribute__((address_space(3))) s16x4* lds_s16x4p;
__device__ __forceinline__ bf16x8 tr_frag(const char* p0, const char* p1) {
  s16x4 a = __builtin_amdgcn_ds_read_tr16_b64_v4i16((lds_s16x4p)p0);
  s16x4 b = __builtin_amdgcn_ds_read_tr16_b64_v4i16((lds_s16x4p)p1);
  s16x8 c = __builtin_shufflevector(a, b, 0, 1, 2, 3, 4, 5, 6, 7);
  return __builtin_bit_cast(bf16x8, c);
}
template <int CTRL>
__device__ __forceinline__ float dppz(float v) {
  return __builtin_bit_cast(float, __builtin_amdgcn_update_dpp(0, __builtin_bit_cast(int, v), CTRL, 0xf, 0xf, true));
}
#define TRRD(dst, addr, off) \
  asm volatile("ds_read_b64_tr_b16 %0, %1 offset:%2" : "=v"(dst) : "v"(addr), "i"(off))
#define SYNC_ALL asm volatile("s_waitcnt vmcnt(0) lgkmcnt(0)\n\ts_barrier" ::: "memory")
#define SYNC_L asm volatile("s_waitcnt lgkmcnt(0)\n\ts_barrier" ::: "memory")

__global__ __launch_bounds__(512, 2) void k1_conv(const u16* __restrict__ ssmk, const u16* __restrict__ ssmD,
                                                  const u16* __restrict__ kvT, const u16* __restrict__ xT,
                                                  const f32x2* __restrict__ st1, const float* __restrict__ modp,
                                                  const u16* __restrict__ w1, const u16* __restrict__ w3,
                                                  u16* __restrict__ x1T, u16* __restrict__ apT,
                                                  float* __restrict__ modf, float* __restrict__ c12) {
  extern __shared__ __attribute__((aligned(16))) char smem[];
  const int tid = threadIdx.x, lane = tid & 63;
  const int w = __builtin_amdgcn_readfirstlane(tid >> 6);
  const int c = w & 3, h = w >> 2;
  const int ch = ((blockIdx.x & 7) + 8 * (blockIdx.x >> 7)) * 64 + ((blockIdx.x >> 3) & 15) * 4 + c;
  char* wb = smem + c * K1_WSZ;
  {
    const u16* src = (h ? kvT : ssmk) + (size_t)ch * S_ + lane * 8;
    char* dst = wb + (h ? K1_UOFF + 256 : K1_KOFF + 128);
#pragma unroll
    for (int i = 0; i < 4; ++i) GLDS16(src + i * 512, dst + i * 1024);
    SCHED;
#pragma unroll
    for (int i = 0; i < 2; ++i) {
      GLDS16(xT + (size_t)ch * S_ + (2 * h + i) * 512 + lane * 8, wb + K1_XOFF + (2 * h + i) * 1024);
      GLDS16((const char*)st1 + (w * 2 + i) * 1024 + lane * 16, smem + K1_STOFF + (w * 2 + i) * 1024);
    }
  }
  SCHED;
  float mv0, mv1;
  u32 ddu;
  {
    const u32 o0 = ((lane & 15) * (6 * D_) + (lane >> 4) * D_) * 4;
    const u32 o1 = ((lane & 15) * (6 * D_) + (4 + ((lane >> 4) & 1)) * D_) * 4;
    asm volatile("global_load_dword %0, %1, %2" : "=v"(mv0) : "v"(o0), "s"(modp + ch) : "memory");
    asm volatile("global_load_dword %0, %1, %2" : "=v"(mv1) : "v"(o1), "s"(modp + ch) : "memory");
    asm volatile("global_load_ushort %0, %1, %2" : "=v"(ddu) : "v"(0u), "s"(ssmD + ch) : "memory");
  }
  const int nt = (blockIdx.x & 7) * 4 + ((blockIdx.x >> 3) & 3), ks = blockIdx.x >> 5;
  f32x4 ms0, ms1;
  {
    const u32 o = ((lane & 15) * (6 * D_) + (lane >> 4) * 4) * 4;
    const float* mb = modp + 3 * D_ + ks * 128 + w * 16;
    asm volatile("global_load_dwordx4 %0, %1, %2" : "=v"(ms0) : "v"(o), "s"(mb + D_) : "memory");
    asm volatile("global_load_dwordx4 %0, %1, %2" : "=v"(ms1) : "v"(o), "s"(mb) : "memory");
  }
  SCHED;
#define K1_SLAB(i)                                                                                   \
  do {                                                                                               \
    const int u_ = (i) * 64 + lane;                                                                  \
    const int um_ = u_ >= 224 ? u_ - 224 : u_;                                                       \
    const u16* wp_ = (u_ >= 224 ? w3 : w1) + (size_t)(ks * 128 + w * 16) * F_ + nt * 112;            \
    GLDS16((const char*)wp_ + (u32)((um_ / 14) * (F_ * 2) + (um_ % 14) * 16),                        \
           smem + K1_SLOFF + w * 7168 + (i) * 1024);                                                 \
  } while (0)
  const u32 lwb = (u32)(uintptr_t)(__attribute__((address_space(3))) char*)wb;
  {
    const u32 za = lwb + (h ? K1_UOFF + (lane & 15) * 16 : ((lane & 15) < 8 ? K1_KOFF + (lane & 7) * 16 : K1_COFF));
    const u32x4 z4 = {0, 0, 0, 0};
    if (lane < 16) asm volatile("ds_write_b128 %0, %1" ::"v"(za), "v"(z4) : "memory");
  }
  const int G = lane >> 4, q = (lane >> 2) & 3, p = lane & 3;
  const u32 abase = lwb + K1_KOFF + 128 - 96 + 32 * p + 8 * (8 * G + q);
  const u32 bbase = lwb + K1_UOFF + 256 + 96 - 32 + 8 * p - 8 * (8 * G + q) + 128 * h;
  const u32 yb = lwb + K1_YOFF + 4 * (64 * h + lane);
  const u32 cy = lwb + K1_COFF + 16 * (h + 1);
  const u32 lds0 = (u32)(uintptr_t)(__attribute__((address_space(3))) char*)smem;
  const u32 mva = lds0 + K1_MVOFF + (w >> 1) * 128 + G * 16 + (w & 1) * 8;
  asm volatile("s_waitcnt vmcnt(9) lgkmcnt(0)\n\ts_barrier" ::: "memory");

  u32x2 Af[16][2], Bf[16][2];
#define K1_RD(a)                               \
  do {                                         \
    TRRD(Af[a][0], abase, 256 * (a));          \
    TRRD(Af[a][1], abase, 256 * (a) + 32);     \
    TRRD(Bf[a][0], bbase, 256 * (a) + 32);     \
    TRRD(Bf[a][1], bbase, 256 * (a));          \
  } while (0)
  K1_RD(0); K1_RD(1); K1_RD(2);
  f32x4 zp = {0, 0, 0, 0};
  float sa, ca1, ga, cb1, dd;
#pragma unroll
  for (int a = 0; a < 17; ++a) {
    if (a < 16) {
      const int n = a < 2 ? 8 : a == 2 ? 10 : a == 3 ? 12 : a < 14 ? 14 : a == 14 ? 10 : 6;
      asm volatile("s_waitcnt lgkmcnt(%0)" ::"i"(n) : "memory");
    }
    SCHED;
    if (a + 3 < 16) K1_RD(a + 3);
    if (a >= 1 && a < 8) { K1_SLAB(a - 1); SCHED; }
    f32x4 z = {0, 0, 0, 0};
    if (a < 16) {
#pragma unroll
      for (int i = 0; i <= a; ++i) {
        u32x4 a4 = {Af[i][0][0], Af[i][0][1], Af[i][1][0], Af[i][1][1]};
        u32x4 b4 = {Bf[a - i][0][0], Bf[a - i][0][1], Bf[a - i][1][0], Bf[a - i][1][1]};
        z = __builtin_amdgcn_mfma_f32_16x16x32_bf16(__builtin_bit_cast(bf16x8, a4), __builtin_bit_cast(bf16x8, b4),
                                                    z, 0, 0, 0);
      }
    }
    if (a > 0) {
      float w2 = dppz<0x138>(zp[3]) + zp[2];
      float w1 = dppz<0x138>(w2) + zp[1];
      float y = dppz<0x138>(w1) + zp[0];
      f32x4 cv = {w1, w2, zp[3], 0.f};
      asm volatile("ds_write_b32 %0, %1 offset:%2" ::"v"(yb), "v"(y), "i"(512 * (a - 1)) : "memory");
      asm volatile("s_mov_b64 exec, %3\n\tds_write_b128 %0, %1 offset:%2\n\ts_mov_b64 exec, -1"
                   ::"v"(cy), "v"(cv), "i"(32 * (a - 1)), "s"(0x8000000000000000ull) : "memory");
    }
    if (a == 12) {
      asm volatile("s_waitcnt vmcnt(7)" : "+v"(mv0), "+v"(mv1), "+v"(ddu), "+v"(ms0), "+v"(ms1) : : "memory");
#define RSUM16(v) do { v += dppz<0xB1>(v); v += dppz<0x4E>(v); v += dppz<0x141>(v); v += dppz<0x140>(v); } while (0)
      RSUM16(mv0);
      RSUM16(mv1);
      mv0 = rbf(mv0);
      mv1 = rbf(mv1);
      const float m0p = rbf(1.0f + mv0), m1p = rbf(1.0f + mv1);
#define RDL(v, l) __builtin_bit_cast(float, __builtin_amdgcn_readlane(__builtin_bit_cast(int, v), l))
      sa = RDL(mv0, 0); ca1 = RDL(m0p, 16); ga = RDL(mv0, 32); cb1 = RDL(m1p, 0);
      dd = bflo(ddu);
      if (h == 0 && lane == 16) modf[ch] = mv1;
    }
    if (a == 13 || a == 14) {
      f32x4 m = a == 13 ? ms0 : ms1;
#pragma unroll
      for (int r = 0; r < 4; ++r) {
        float t = m[r];
        RSUM16(t);
        if (a == 13) t = 1.0f + rbf(t);
        m[r] = t;
      }
      u32x2 mv = {pk2(m[0], m[1]), pk2(m[2], m[3])};
      asm volatile("s_mov_b64 exec, %3\n\tds_write_b64 %0, %1 offset:%2\n\ts_mov_b64 exec, -1"
                   ::"v"(mva), "v"(mv), "i"(64 * (a - 13)), "s"(0x0001000100010001ull) : "memory");
    }
    zp = z;
    SCHED;
  }
  SYNC_L;
#pragma unroll
  for (int it = 0; it < 2; ++it) {
    const int so = h * 1024 + it * 512 + lane * 8;
    f32x4 y0 = *(const f32x4*)(wb + K1_YOFF + so * 4);
    f32x4 y1 = *(const f32x4*)(wb + K1_YOFF + so * 4 + 16);
    f32x4 cv = *(const f32x4*)(wb + K1_COFF + ((lane & 7) ? 0 : (so >> 6) * 16));
    u32x4 uv = *(const u32x4*)(wb + K1_UOFF + 256 + so * 2);
    u32x4 xv = *(const u32x4*)(wb + K1_XOFF + so * 2);
    y0 += cv;
    u32x4 o1, o2;
#pragma unroll
    for (int j = 0; j < 4; ++j) {
      f32x4 st = *(const f32x4*)(smem + K1_STOFF + (so + 2 * j) * 8);
      float r[2], ap[2];
#pragma unroll
      for (int e = 0; e < 2; ++e) {
        float xf = e ? bfhi(xv[j]) : bflo(xv[j]);
        float yf = j < 2 ? y0[2 * j + e] : y1[2 * j - 4 + e];
        float uf = e ? bfhi(uv[j]) : bflo(uv[j]);
        float m1 = st[2 * e], r1 = st[2 * e + 1];
        float h1 = fmaf((xf - m1) * r1, ca1, sa);
        float yy = fmaf(uf, dd, yf);
        r[e] = fmaf(ga, h1 * yy, xf);
      }
      o1[j] = pk2(r[0], r[1]);
      ap[0] = (bflo(o1[j]) - st[0]) * cb1;
      ap[1] = (bfhi(o1[j]) - st[2]) * cb1;
      o2[j] = pk2(ap[0], ap[1]);
    }
    ST16(x1T + (size_t)ch * S_ + so, o1, STP1);
    ST16(apT + (size_t)ch * S_ + so, o2, STP1);
  }
  SCHED;
  asm volatile("s_waitcnt vmcnt(4)\n\ts_barrier" ::: "memory");
  {
    const int f1 = w < 6 ? w + 8 : w;
    const u32 ra = lds0 + K1_SLOFF + (4 * G + q) * 224 + p * 8;
    const u32 a0 = ra + (w >= 7 ? 3584 + (w - 7) * 32 : w * 32);
    const u32 a1 = ra + (f1 >= 7 ? 3584 + (f1 - 7) * 32 : f1 * 32);
    const u32 mb = lds0 + K1_MVOFF + (lane & 1) * 64 + G * 16;
    u32x2 fa[4][2], fb[4][2];
    u32x4 mf[4];
#pragma unroll
    for (int k = 0; k < 4; ++k) {
      asm volatile("ds_read_b128 %0, %1 offset:%2" : "=v"(mf[k]) : "v"(mb), "i"(128 * k));
      TRRD(fa[k][0], a0, 14336 * k);
      TRRD(fa[k][1], a0, 14336 * k + 7168);
      TRRD(fb[k][0], a1, 14336 * k);
      TRRD(fb[k][1], a1, 14336 * k + 7168);
    }
    WAIT_L(0);
    SCHED;
    f32x4 z0 = {0, 0, 0, 0}, z1 = {0, 0, 0, 0};
#pragma unroll
    for (int k = 0; k < 4; ++k) {
      u32x4 x0 = {fa[k][0][0], fa[k][0][1], fa[k][1][0], fa[k][1][1]};
      u32x4 x1 = {fb[k][0][0], fb[k][0][1], fb[k][1][0], fb[k][1][1]};
      z0 = __builtin_amdgcn_mfma_f32_16x16x32_bf16(__builtin_bit_cast(bf16x8, x0), __builtin_bit_cast(bf16x8, mf[k]), z0, 0, 0, 0);
      z1 = __builtin_amdgcn_mfma_f32_16x16x32_bf16(__builtin_bit_cast(bf16x8, x1), __builtin_bit_cast(bf16x8, mf[k]), z1, 0, 0, 0);
    }
    if ((lane & 15) < 2) {
      float* cp = c12 + (ks * 32 + nt) * 448 + (lane & 1) * 224 + 4 * G;
      *(f32x4*)(cp + 16 * w) = z0;
      if (w < 6) *(f32x4*)(cp + 16 * f1) = z1;
    }
  }
}

#define G1_SLOT 32768
#define G1_R 5
#define G1_BOFF 16384
#define G1_LDS (G1_R * G1_SLOT)


typedef __attribute__((address_space(1))) unsigned long long gu64;

__device__ __forceinline__ void wf_merge(float& m, float& q, float ma, float qa, float mb, float qb, float hn) {
  float d = mb - ma;
  m = 0.5f * (ma + mb);
  q = fmaf(d * hn, d, qa + qb);
}
__device__ __forceinline__ void wf_swap32(float& m, float& q, float ma, float qa, float mb, float qb, float hn) {
  auto a = __builtin_amdgcn_permlane32_swap(__float_as_uint(ma), __float_as_uint(mb), false, false);
  auto c = __builtin_amdgcn_permlane32_swap(__float_as_uint(qa), __float_as_uint(qb), false, false);
  wf_merge(m, q, __uint_as_float(a[0]), __uint_as_float(c[0]), __uint_as_float(a[1]), __uint_as_float(c[1]), hn);
}
__device__ __forceinline__ void wf_swap16(float& m, float& q, float ma, float qa, float mb, float qb, float hn) {
  auto a = __builtin_amdgcn_permlane16_swap(__float_as_uint(ma), __float_as_uint(mb), false, false);
  auto c = __builtin_amdgcn_permlane16_swap(__float_as_uint(qa), __float_as_uint(qb), false, false);
  wf_merge(m, q, __uint_as_float(a[0]), __uint_as_float(c[0]), __uint_as_float(a[1]), __uint_as_float(c[1]), hn);
}
template <int CTRL>
__device__ __forceinline__ void wf_dpp(float& m, float& q, float hn) {
  float mo = dppz<CTRL>(m), qo = dppz<CTRL>(q);
  wf_merge(m, q, m, q, mo, qo, hn);
}

__global__ __launch_bounds__(512, 2) void g1_gemm(const u16* __restrict__ apT, const u16* __restrict__ w1,
                                                  const u16* __restrict__ w3, const u16* __restrict__ x1T,
                                                  const f32x2* __restrict__ st1, unsigned long long* st2,
                                                  const float* __restrict__ c12, const u16* __restrict__ w2,
                                                  char* xch, u16* __restrict__ hh) {
  extern __shared__ __attribute__((aligned(16))) char smem[];
  const int tid = threadIdx.x, lane = tid & 63;
  const int w = __builtin_amdgcn_readfirstlane(tid >> 6);
  const int grp = w >> 2;
  const int wm = w & 3, wn = grp;
  const int b = blockIdx.x;
  const int xcd = b & 7, idx = b >> 3;
  const int mt = idx & 7, nt = xcd * 4 + (idx >> 3);
  const int s0 = mt * 256, f0 = nt * 112;

  const u32 lds0 = (u32)(uintptr_t)(__attribute__((address_space(3))) char*)smem;
  u32 voa[2], vob;
#pragma unroll
  for (int j = 0; j < 2; ++j) {
    int row = 4 * w + 2 * j + (lane >> 5);
    int c = (lane & 31) ^ ((row & 7) << 1);
    voa[j] = row * (S_ * 2) + c * 16 - j * 1024;
  }
  {
    int li = lane - 8 * (w & 1);
    li = li < 0 ? 0 : (li > 55 ? 55 : li);
    vob = (4 * w + li / 14) * (F_ * 2) + (li % 14) * 16;
  }
  const unsigned long long bmask = (w & 1) ? 0xFFFFFFFFFFFFFF00ull : 0x00FFFFFFFFFFFFFFull;
  const char* sa_ = (const char*)apT + s0 * 2;
  const char* sb1 = (const char*)w1 + f0 * 2 - 2048;
  const char* sb3 = (const char*)w3 + f0 * 2 - 3072;
  u32 wdst = lds0 + w * 4096;
#define G1_GA(j)                                                                                   \
  asm volatile("s_mov_b32 m0, %0\n\ts_nop 0\n\tglobal_load_lds_dwordx4 %1, %2 offset:%3"           \
               ::"s"(wdst), "v"(voa[j]), "s"(sa_), "i"((j) * 1024) : "memory")
#define GBX "s_mov_b32 m0, %0\n\ts_mov_b64 exec, %4\n\tglobal_load_lds_dwordx4 %1, %2 offset:%3\n\ts_mov_b64 exec, -1"
#define G1_GB(sb, off)                                                                             \
  asm volatile(GBX \
               ::"s"(wdst), "v"(vob), "s"(sb), "i"(off), "s"(bmask) : "memory")
#define G1_ADV                                                                                     \
  do {                                                                                             \
    sa_ += 32 * S_ * 2; sb1 += 32 * F_ * 2; sb3 += 32 * F_ * 2;                                    \
    wdst += G1_SLOT; if (wdst >= lds0 + G1_R * G1_SLOT) wdst -= G1_R * G1_SLOT;                    \
  } while (0)
#pragma unroll
  for (int i = 0; i < G1_R; ++i) {
    G1_GB(sb1, 2048); G1_GB(sb3, 3072); G1_GA(0); G1_GA(1);
    G1_ADV;
  }
  SCHED;

  const int r0 = (xcd * 32 + idx) * 8;
  u32x4 xa, xb;
  float m1v;
  {
    const u32 xo = tid * (S_ * 2);
    const char* xbase0 = (const char*)x1T + r0 * 2;
    const char* xbase1 = xbase0 + 512 * (S_ * 2);
    asm volatile("global_load_dwordx4 %0, %1, %2" : "=v"(xa) : "v"(xo), "s"(xbase0) : "memory");
    asm volatile("global_load_dwordx4 %0, %1, %2" : "=v"(xb) : "v"(xo), "s"(xbase1) : "memory");
    const u32 mo = (lane & 7) * 8;
    asm volatile("global_load_dword %0, %1, %2" : "=v"(m1v) : "v"(mo), "s"(st1 + r0) : "memory");
  }
  SCHED;

  u32 ard[4], brd[2];
  {
    const int g = lane >> 4, q = (lane >> 2) & 3, p = lane & 3;
    const int row = 4 * g + q;
#pragma unroll
    for (int m = 0; m < 4; ++m) {
      int c = (8 * wm + 2 * m + (p >> 1)) ^ ((row & 7) << 1);
      ard[m] = lds0 + g * 4096 + q * 512 + c * 16 + 8 * (p & 1);
    }
    const u32 bb = lds0 + g * 4096 + 2048 + (g & 1) * 128 + q * 224 + 7 * wn * 16 + 8 * (p & 1);
    brd[0] = bb + (p >> 1) * 16;
    brd[1] = bb + (p >> 1) * 1024 + 96;
  }

  f32x4 acc[7][4];
  u32x2 fa[2][4][2], fb[2][7][2];
  u32 rso = 0;
  u32 ra[4], rb[2];
#define G1_RDX(TR, st, i)                                                                            \
  do {                                                                                             \
    if ((i) < 14) {                                                                                \
      const int n_ = (i) >> 1;                                                                     \
      TR(fb[st][n_][(i) & 1], rb[n_ < 6 ? 0 : 1],                                                \
           (n_ < 6 ? (n_ & 1) * 1024 + (n_ >> 1) * 32 : 0) + ((i) & 1) * 16384);                   \
    } else {                                                                                       \
      TR(fa[st][((i) - 14) >> 1][(i) & 1], ra[((i) - 14) >> 1], ((i) & 1) * 16384);              \
    }                                                                                              \
  } while (0)
#define G1_RD(st, i) G1_RDX(TRRD, st, i)
#define TRRD2(dst, addr, off) \
  asm volatile("ds_read_b64_tr_b16 %0, %1 offset:%2" : "+v"(dst) : "v"(addr), "i"(off))
#define G1_RADDR                                                                                   \
  do {                                                                                             \
    _Pragma("unroll") for (int m = 0; m < 4; ++m) ra[m] = ard[m] + rso;                            \
    rb[0] = brd[0] + rso; rb[1] = brd[1] + rso;                                                    \
    rso += G1_SLOT; if (rso == G1_R * G1_SLOT) rso = 0;                                            \
    asm volatile("" : "+s"(rso));                                                                  \
  } while (0)
#define G1_MM(Z, st, i)                                                                            \
  do {                                                                                             \
    const int m_ = (i) / 7, n_ = (i) % 7;                                                          \
    u32x4 a4 = {fa[st][m_][0][0], fa[st][m_][0][1], fa[st][m_][1][0], fa[st][m_][1][1]};           \
    u32x4 b4 = {fb[st][n_][0][0], fb[st][n_][0][1], fb[st][n_][1][0], fb[st][n_][1][1]};           \
    if (Z) asm volatile("v_mfma_f32_16x16x32_bf16 %0, %1, %2, 0" : "=&v"(acc[n_][m_]) : "v"(b4), "v"(a4)); \
    else asm volatile("v_mfma_f32_16x16x32_bf16 %0, %1, %2, %0" : "+v"(acc[n_][m_]) : "v"(b4), "v"(a4)); \
  } while (0)
#define G1_LD(TR, st, STG)                                                                         \
  do {                                                                                             \
    _Pragma("unroll") for (int i = 0; i < 22; ++i) G1_RDX(TR, 1 - (st), i);                        \
    if (STG) { G1_GB(sb1, 2048); G1_GB(sb3, 3072); G1_GA(0); G1_GA(1); G1_ADV; }                   \
    G1_RADDR;                                                                                      \
    SCHED;                                                                                         \
  } while (0)
#define G1_IT(MODE, st, VM, STG, HOOK, EXTRA)                                                      \
  do {                                                                                             \
    SCHED;                                                                                         \
    asm volatile("s_waitcnt vmcnt(" #VM ") lgkmcnt(0)\n\ts_barrier" ::: "memory");                 \
    SCHED;                                                                                         \
    EXTRA;                                                                                         \
    if (grp) G1_LD(TRRD, st, STG);                                                                 \
    SCHED;                                                                                         \
    _Pragma("unroll") for (int i = 0; i < 28; ++i) {                                               \
      G1_MM(MODE, st, i);                                                                            \
      SCHED;                                                                                       \
      HOOK;                                                                                        \
      SCHED;                                                                                       \
    }                                                                                              \
    if (!grp) G1_LD(TRRD2, st, STG);                                                               \
    SCHED;                                                                                         \
  } while (0)

  G1_RADDR;
  SCHED;
  asm volatile("s_waitcnt vmcnt(19)\n\ts_barrier" ::: "memory");
  SCHED;
#pragma unroll
  for (int i = 0; i < 22; ++i) G1_RD(0, i);
  G1_RADDR;
  SCHED;
  unsigned long long sg[4];
  int tid2;
  gu64* sp;
#define G1_MAIN(MODE)                                                                              \
  do {                                                                                             \
    G1_IT(1, 0, 15, 1, {}, {});                                                                    \
    G1_IT(MODE, 1, 15, 1, {}, {});                                                                 \
    G1_IT(MODE, 0, 15, 1, {}, {});                                                                 \
    G1_IT(MODE, 1, 15, 1, {}, {});                                                                 \
    float smm, sqq, sm[8], sq[8];                                                                  \
    G1_IT(MODE, 0, 12, 1, {                                                                        \
      if (i == 0) { asm volatile("" : "+v"(xa), "+v"(xb), "+v"(m1v) : : "memory"); }               \
      if (i < 8) wf_merge(sm[i], sq[i], (i & 1) ? bfhi(xa[i >> 1]) : bflo(xa[i >> 1]), 0.f,        \
                          (i & 1) ? bfhi(xb[i >> 1]) : bflo(xb[i >> 1]), 0.f, 0.5f);               \
      else if (i < 12)                                                                             \
        wf_swap32(sm[i & 3], sq[i & 3], sm[i & 3], sq[i & 3], sm[(i & 3) + 4], sq[(i & 3) + 4], 1.f); \
      else if (i < 14)                                                                             \
        wf_swap16(sm[i & 1], sq[i & 1], sm[i & 1], sq[i & 1], sm[(i & 1) + 2], sq[(i & 1) + 2], 2.f); \
      else if (i == 14) {                                                                          \
        const bool hi8 = lane & 8;                                                                 \
        float mo = dppz<0x128>(hi8 ? sm[0] : sm[1]);                                               \
        float qo = dppz<0x128>(hi8 ? sq[0] : sq[1]);                                               \
        wf_merge(smm, sqq, hi8 ? sm[1] : sm[0], hi8 ? sq[1] : sq[0], mo, qo, 4.f);                 \
      } else if (i == 15) wf_dpp<0xB1>(smm, sqq, 8.f);                                             \
      else if (i == 16) wf_dpp<0x4E>(smm, sqq, 16.f);                                              \
      else if (i == 17) wf_dpp<0x141>(smm, sqq, 32.f);                                             \
      else if (i == 18) {                                                                          \
               \
        u32x2 v;                                                                                   \
        v[0] = __float_as_uint(smm);                                                               \
        v[1] = __float_as_uint(sqq);                                                               \
        u32 ad = lds0 + w * 4096 + 2048 + ((w & 1) ? 0 : 896) + lane;                              \
        asm volatile("s_mov_b64 exec, %2\n\tds_write_b64 %0, %1\n\ts_mov_b64 exec, -1"             \
                     ::"v"(ad), "v"(v), "s"(0x0101010101010101ull) : "memory");                    \
      }                                                                                            \
    }, {});                                                                                        \
    G1_IT(MODE, 1, 12, 1, {}, {                                                                    \
      if (w == 0) {                                                                                \
        u32x2 v;                                                                                   \
        int l1 = lane;                                                                             \
        asm volatile("" : "+v"(l1));                                                               \
        u32 ad = lds0 + (l1 >> 3) * 4096 + 2048 + ((l1 & 8) ? 0 : 896) + (l1 & 7) * 8;             \
        asm volatile("ds_read_b64 %0, %1\n\ts_waitcnt lgkmcnt(0)" : "=v"(v) : "v"(ad) : "memory"); \
        float mm = __uint_as_float(v[0]);                                                          \
        float qq = __uint_as_float(v[1]);                                                          \
        wf_dpp<0x128>(mm, qq, 64.f);                                                               \
        wf_swap16(mm, qq, mm, qq, mm, qq, 128.f);                                                  \
        wf_swap32(mm, qq, mm, qq, mm, qq, 256.f);                                                  \
        float r2 = rsqrtf(qq * (1.0f / D_) + 1e-5f);                                               \
        float rd = r2 * (mm - m1v);                                                                \
        u32 rb_ = __float_as_uint(r2);                                                             \
        if (rb_ == 0u) rb_ = 0x80000000u;                                                          \
        if (l1 < 8)                                                                                \
          __hip_atomic_store((gu64*)(st2 + r0 + l1),                                               \
                             ((unsigned long long)__float_as_uint(rd) << 32) | rb_,                \
                             __ATOMIC_RELAXED, __HIP_MEMORY_SCOPE_AGENT);                          \
      }                                                                                            \
      SCHED;                                                                                       \
    });                                                                                            \
    u32 pfd;                                                                                       \
    {                                                                                              \
            \
      const u32 po = (tid < 448 ? tid : 447) * 64;                                                 \
      asm volatile("global_load_dword %0, %1, %2"                                                  \
                   : "=v"(pfd) : "v"(po), "s"((const char*)w2 + (size_t)b * 28672) : "memory");    \
    }                                                                                              \
    const char* xf = xch + (size_t)b * 20480;                                                      \
    const u32 xfo = tid * 4;                                                                       \
    const u32 ones = 0xFFFFFFFFu;                                                                  \
    for (int t = 6; t < 26; t += 2) {                                                              \
      G1_IT(MODE, 0, 12, 1, {}, {                                  \
        asm volatile("global_store_dword %0, %1, %2 " XFL ::"v"(xfo), "v"(ones), "s"(xf) : "memory"); \
        xf += 2048;                                                                                \
        SCHED;                                                                                     \
      });                                                                                          \
      G1_IT(MODE, 1, 12, 1, {}, {});                                                               \
    }                                                                                              \
    asm volatile("" ::"v"(pfd));                                                                   \
    tid2 = tid;                                                                                    \
    asm volatile("" : "+v"(tid2));                                                                 \
    float cp[8];                                                                                   \
    G1_IT(MODE, 0, 12, 1, {}, {                                      \
      const u32 co = (tid2 < 448 ? tid2 : 447) * 4;                                                \
      _Pragma("unroll") for (int ks = 0; ks < 8; ++ks)                                             \
        asm volatile("global_load_dword %0, %1, %2"                                                \
                     : "=v"(cp[ks]) : "v"(co), "s"(c12 + (ks * 32 + nt) * 448) : "memory");        \
      SCHED;                                                                                       \
    });                                                                                            \
    sp = (gu64*)(st2 + s0 + wm * 64 + (tid2 & 15));                                                \
    G1_IT(MODE, 1, 20, 0, {}, {                                                                    \
      _Pragma("unroll") for (int m = 0; m < 4; ++m)                                                \
        asm volatile("global_load_dwordx2 %0, %1, off offset:%2 sc1"                               \
                     : "=v"(sg[m]) : "v"(sp), "i"(128 * m) : "memory");                            \
      SCHED;                                                                                       \
    });                                                                                            \
    G1_IT(MODE, 0, 20, 0, {}, {});                                                                 \
    G1_IT(MODE, 1, 8, 0, {}, {                       \
      asm volatile("" : "+v"(cp[0]), "+v"(cp[1]), "+v"(cp[2]), "+v"(cp[3]), "+v"(cp[4]),           \
                        "+v"(cp[5]), "+v"(cp[6]), "+v"(cp[7]) : : "memory");                       \
      float csum = ((cp[0] + cp[1]) + (cp[2] + cp[3])) + ((cp[4] + cp[5]) + (cp[6] + cp[7]));      \
      u32 ad = lds0 + 2 * G1_SLOT + tid2 * 4;                                                      \
      asm volatile("ds_write_b32 %0, %1" ::"v"(ad), "v"(csum) : "memory");                         \
      SCHED;                                                                                       \
    });                                                                                            \
    G1_IT(MODE, 0, 4, 0, {}, {});                                                                  \
    asm volatile("s_waitcnt vmcnt(0) lgkmcnt(0)"                                                   \
                 : "+v"(sg[0]), "+v"(sg[1]), "+v"(sg[2]), "+v"(sg[3]) : : "memory");               \
    SCHED;                                                                                         \
    _Pragma("unroll") for (int i = 0; i < 28; ++i) G1_MM(0, 1, i);                                   \
    asm volatile("s_nop 7\n\ts_nop 7" ::: "memory");                                               \
    SCHED;                                                                                         \
  } while (0)
  G1_MAIN(0);
  const int lane2 = tid2 & 63, g = lane2 >> 4;

  {
    for (int spin = 0; spin < (1 << 22); ++spin) {
      bool ok = ((u32)sg[0] != 0u) & ((u32)sg[1] != 0u) & ((u32)sg[2] != 0u) & ((u32)sg[3] != 0u);
      if (__all(ok)) break;
      __builtin_amdgcn_s_sleep(8);
#pragma unroll
      for (int m = 0; m < 4; ++m) sg[m] = __hip_atomic_load(sp + 16 * m, __ATOMIC_RELAXED, __HIP_MEMORY_SCOPE_AGENT);
    }
    float r2[4], rd[4];
#pragma unroll
    for (int m = 0; m < 4; ++m) {
      r2[m] = __uint_as_float((u32)sg[m]);
      rd[m] = -__uint_as_float((u32)(sg[m] >> 32));
    }
    const float* cl = (const float*)(smem + 2 * G1_SLOT) + wn * 56;
    char* ob = (char*)hh + ((size_t)((s0 + wm * 64) >> 4) * 448 + nt * 14 + wn * 7) * 256 + (lane2 & 15) * 16;
#pragma unroll
    for (int j = 0; j < 3; ++j) {
      const f32x4 c1g = *(const f32x4*)(cl + 16 * j + 4 * g), c1u = *(const f32x4*)(cl + 112 + 16 * j + 4 * g);
      const f32x4 c2g = *(const f32x4*)(cl + 224 + 16 * j + 4 * g), c2u = *(const f32x4*)(cl + 336 + 16 * j + 4 * g);
      u32 o[4][2];
#pragma unroll
      for (int m = 0; m < 4; ++m) {
        float hv[4];
#pragma unroll
        for (int r = 0; r < 4; ++r) {
          float gt = fmaf(acc[2 * j][m][r], r2[m], fmaf(rd[m], c1g[r], c2g[r]));
          float up = fmaf(acc[2 * j + 1][m][r], r2[m], fmaf(rd[m], c1u[r], c2u[r]));
          hv[r] = silu_f(gt) * up;
        }
        o[m][0] = pk2(hv[0], hv[1]);
        o[m][1] = pk2(hv[2], hv[3]);
      }
#pragma unroll
      for (int mp = 0; mp < 2; ++mp) {
        auto d0 = __builtin_amdgcn_permlane16_swap(o[2 * mp][0], o[2 * mp + 1][0], false, false);
        auto d1 = __builtin_amdgcn_permlane16_swap(o[2 * mp][1], o[2 * mp + 1][1], false, false);
        u32x4 ov = {d0[0], d1[0], d0[1], d1[1]};
        ST16(ob + ((size_t)(2 * mp + (g & 1)) * 448 + 2 * j + (g >> 1)) * 256, ov, STPG);
      }
    }
    {
      const int co = (g >> 1) * 112 + 48 + 4 * (g & 1);
      const f32x4 c1 = *(const f32x4*)(cl + co), c2 = *(const f32x4*)(cl + 224 + co);
      u32 o[2][2];
#pragma unroll
      for (int mm = 0; mm < 2; ++mm) {
        float hv[4];
#pragma unroll
        for (int r = 0; r < 4; ++r) {
          float va = fmaf(acc[6][mm][r], r2[mm], fmaf(rd[mm], c1[r], c2[r]));
          float vb = fmaf(acc[6][mm + 2][r], r2[mm + 2], fmaf(rd[mm + 2], c1[r], c2[r]));
          auto sw = __builtin_amdgcn_permlane32_swap(__float_as_uint(va), __float_as_uint(vb), false, false);
          hv[r] = silu_f(__uint_as_float(sw[0])) * __uint_as_float(sw[1]);
        }
        o[mm][0] = pk2(hv[0], hv[1]);
        o[mm][1] = pk2(hv[2], hv[3]);
      }
      auto d0 = __builtin_amdgcn_permlane16_swap(o[0][0], o[1][0], false, false);
      auto d1 = __builtin_amdgcn_permlane16_swap(o[0][1], o[1][1], false, false);
      u32x4 ov = {d0[0], d1[0], d0[1], d1[1]};
      ST16(ob + ((size_t)g * 448 + 6) * 256, ov, STPG);
    }
  }
}

#define G2_BSLOT 8192
#define G2_XOFF 98304
#define G2_LDS (G2_XOFF + 65536)

__global__ __launch_bounds__(256, 1) void g2_gemm(const u16* __restrict__ hh, const u16* __restrict__ w2,
                                                  const u16* __restrict__ x1T, const float* __restrict__ modf,
                                                  char* xch, u16* __restrict__ out) {
  extern __shared__ __attribute__((aligned(16))) char smem[];
  const int tid = threadIdx.x, lane = tid & 63;
  const int w = __builtin_amdgcn_readfirstlane(tid >> 6);
  const int b = blockIdx.x;
  const int xcd = b & 7, idx = b >> 3;
  const int kh = xcd & 1;
  const int mt = (xcd >> 1) * 4 + (idx & 3), nt = idx >> 2;
  const int s0 = mt * 128, d0 = nt * 128;
  const u32 lds0 = (u32)(uintptr_t)(__attribute__((address_space(3))) char*)smem;

  u32 boff[8];
#pragma unroll
  for (int i = 0; i < 8; ++i) {
    const int f = 8 * (i & 3) + 4 * (i >> 2) + (lane >> 4);
    const int c = (lane & 15) ^ ((4 * (i & 1) + (lane >> 4)) << 1);
    boff[i] = (u32)(f * (D_ * 2) + c * 16 + 3072 - (i & 3) * 1024);
  }
  const char* bbase = (const char*)w2 + (size_t)(1792 * kh + 32 * w) * (D_ * 2) + d0 * 2 - 3072;
  const u32 bdst = lds0 + w * (3 * G2_BSLOT);
  const char* ab[8];
#pragma unroll
  for (int m = 0; m < 8; ++m)
    ab[m] = (const char*)hh + (size_t)((s0 >> 4) + 4 * ((m >> 2) ^ kh ^ 1) + ((m + w) & 3)) * (448 * 256);
  u32 aoff = kh * 57344 + w * 1024 + lane * 16;
#define G2_LDA(set, m) \
  asm volatile("global_load_dwordx4 %0, %1, %2" : "=v"(A[set][m]) : "v"(aoff), "s"(ab[m]) : "memory")
#define G2_LDB2(slot, i)                                                     \
  asm volatile("s_add_u32 m0, %0, %1\n\ts_nop 0\n\tglobal_load_lds_dwordx4 %2, %4 offset:%5\n\t"     \
               "global_load_lds_dwordx4 %3, %4 offset:%6"                                            \
               ::"s"(bdst), "i"((slot) * G2_BSLOT + ((i) >> 2) * 4096), "v"(boff[i]), "v"(boff[(i) + 1]), \
                 "s"(bbase), "i"(((i) & 3) * 1024), "i"(((i) & 3) * 1024 + 1024) : "memory", "scc")
#define G2_ADV do { aoff += 4096; bbase += (size_t)128 * D_ * 2; } while (0)

  f32x4 acc[8][8];
  u32x4 A[3][8];
  u32x2 bf[2][8][2];
#define G2_PRO(set)                                               \
  do {                                                            \
    _Pragma("unroll") for (int m = 0; m < 8; ++m) {               \
      G2_LDA(set, m);                                             \
      if (m < 4) G2_LDB2(set, 2 * m);                             \
    }                                                             \
    SCHED;                                                        \
    G2_ADV;                                                       \
  } while (0)
  G2_PRO(0); G2_PRO(1);
#pragma unroll
  for (int i = 0; i < 4; ++i)
    GLDS16((const char*)x1T + (size_t)(d0 + 32 * i + (lane >> 1)) * (S_ * 2) + (s0 + 64 * kh + 16 * w) * 2 + (lane & 1) * 16,
           smem + G2_XOFF + w * 16384 + i * 1024);
  SCHED;
  G2_PRO(2);

  const int g = lane >> 4, q = (lane >> 2) & 3, p = lane & 3;
  u32 brd[8];
  {
    const int row = 4 * g + q;
#pragma unroll
    for (int n = 0; n < 8; ++n) brd[n] = bdst + row * 256 + ((n ^ (row & 7)) * 32) + p * 8;
  }
#define G2_RDB(bs, slot, n)                                                \
  do {                                                                     \
    TRRD(bf[bs][n][0], brd[n], (slot) * G2_BSLOT);                         \
    TRRD(bf[bs][n][1], brd[n], (slot) * G2_BSLOT + 4096);                  \
  } while (0)
  WAIT_V(40);
#pragma unroll
  for (int n = 0; n < 8; ++n) G2_RDB(0, 0, n);
#define G2_B4(bs, n) ((u32x4){bf[bs][n][0][0], bf[bs][n][0][1], bf[bs][n][1][0], bf[bs][n][1][1]})
#define G2_BOPS(bs)                                                                                  \
  "v"(G2_B4(bs, 0)), "v"(G2_B4(bs, 1)), "v"(G2_B4(bs, 2)), "v"(G2_B4(bs, 3)), "v"(G2_B4(bs, 4)),     \
      "v"(G2_B4(bs, 5)), "v"(G2_B4(bs, 6)), "v"(G2_B4(bs, 7))
#define G2_ACC(C, m)                                                                                 \
  C(acc[0][m]), C(acc[1][m]), C(acc[2][m]), C(acc[3][m]), C(acc[4][m]), C(acc[5][m]), C(acc[6][m]), C(acc[7][m])
#define G2_MFMA8                                           \
  "v_mfma_f32_16x16x32_bf16 %0, %9, %8, %0\n\t"            \
  "v_mfma_f32_16x16x32_bf16 %1, %10, %8, %1\n\t"           \
  "v_mfma_f32_16x16x32_bf16 %2, %11, %8, %2\n\t"           \
  "v_mfma_f32_16x16x32_bf16 %3, %12, %8, %3\n\t"           \
  "v_mfma_f32_16x16x32_bf16 %4, %13, %8, %4\n\t"           \
  "v_mfma_f32_16x16x32_bf16 %5, %14, %8, %5\n\t"           \
  "v_mfma_f32_16x16x32_bf16 %6, %15, %8, %6\n\t"           \
  "v_mfma_f32_16x16x32_bf16 %7, %16, %8, %7\n\t"
#define G2_MFMA8Z                                          \
  "v_mfma_f32_16x16x32_bf16 %0, %9, %8, 0\n\t"             \
  "v_mfma_f32_16x16x32_bf16 %1, %10, %8, 0\n\t"            \
  "v_mfma_f32_16x16x32_bf16 %2, %11, %8, 0\n\t"            \
  "v_mfma_f32_16x16x32_bf16 %3, %12, %8, 0\n\t"            \
  "v_mfma_f32_16x16x32_bf16 %4, %13, %8, 0\n\t"            \
  "v_mfma_f32_16x16x32_bf16 %5, %14, %8, 0\n\t"            \
  "v_mfma_f32_16x16x32_bf16 %6, %15, %8, 0\n\t"            \
  "v_mfma_f32_16x16x32_bf16 %7, %16, %8, 0\n\t"
#define G2_MML(set, bs, m, WV)                                                                       \
  asm volatile("s_waitcnt vmcnt(%19)\n\t" G2_MFMA8 "global_load_dwordx4 %8, %17, %18"                \
               : G2_ACC("+a", m), "+v"(A[set][m])                                                    \
               : G2_BOPS(bs), "v"(aoff), "s"(ab[m]), "i"(WV) : "memory")
#define G2_MML0(set, bs, m, WV)                                                                      \
  asm volatile("s_waitcnt vmcnt(%19)\n\t" G2_MFMA8Z "global_load_dwordx4 %8, %17, %18"               \
               : G2_ACC("=a", m), "+v"(A[set][m])                                                    \
               : G2_BOPS(bs), "v"(aoff), "s"(ab[m]), "i"(WV) : "memory")
#define G2_MM(set, bs, m, WV)                                                                        \
  asm volatile("s_waitcnt vmcnt(%17)\n\t" G2_MFMA8                                                   \
               : G2_ACC("+a", m) : "v"(A[set][m]), G2_BOPS(bs), "i"(WV) : "memory")
#define G2_RDN(set, bs, m)                                                                    \
  do {                                                                                        \
    if ((m) >= 4 && (m) < 7) {                                                                \
      G2_RDB((bs) ^ 1, ((set) + 1) % 3, 3 * ((m) - 4));                                       \
      G2_RDB((bs) ^ 1, ((set) + 1) % 3, 3 * ((m) - 4) + 1);                                   \
      if ((m) < 6) G2_RDB((bs) ^ 1, ((set) + 1) % 3, (3 * ((m) - 4) + 2) & 7);                \
    }                                                                                         \
  } while (0)
#define G2_STEP(set, bs, MML)                                                                 \
  do {                                                                                        \
    WAIT_L(0);                                                                                \
    SCHED;                                                                                    \
    _Pragma("unroll") for (int m = 0; m < 8; ++m) {                                           \
      MML(set, bs, m, 47);                                                                    \
      if (m < 4) G2_LDB2(set, 2 * (m & 3));                                                   \
      if (m == 4) WAIT_V(33);                                                                 \
      G2_RDN(set, bs, m);                                                                     \
      SCHED;                                                                                  \
    }                                                                                         \
    G2_ADV;                                                                                   \
  } while (0)
#define G2_TAIL(set, bs, NEW, NEXT)                                                           \
  do {                                                                                        \
    WAIT_L(0);                                                                                \
    SCHED;                                                                                    \
    _Pragma("unroll") for (int m = 0; m < 8; ++m) {                                           \
      G2_MM(set, bs, m, (NEW) + 15 - (m < 4 ? 3 * m : 8 + m));                                \
      if (NEXT) {                                                                             \
        if (m == 4) asm volatile("s_waitcnt vmcnt(%0)" ::"i"((NEXT) ? (NEW) - 12 : 0) : "memory"); \
        G2_RDN(set, bs, m);                                                                   \
      }                                                                                       \
      SCHED;                                                                                  \
    }                                                                                         \
  } while (0)
  G2_STEP(0, 0, G2_MML0);
  G2_STEP(1, 1, G2_MML);
  G2_STEP(2, 0, G2_MML);
  G2_STEP(0, 1, G2_MML);
  G2_STEP(1, 0, G2_MML);
  G2_STEP(2, 1, G2_MML);
  G2_STEP(0, 0, G2_MML);
  G2_STEP(1, 1, G2_MML);
  G2_STEP(2, 0, G2_MML);
  G2_STEP(0, 1, G2_MML);
  G2_STEP(1, 0, G2_MML);
  u32x2 xu[8];
  {
    const u32 xb = lds0 + G2_XOFF + w * 16384 + (4 * g + q) * 32 + p * 8;
#pragma unroll
    for (int n = 0; n < 8; ++n) TRRD(xu[n], xb, n * 512);
  }
  G2_TAIL(2, 1, 32, true);
  const int tile = mt * 8 + nt;
  const char* xin = xch + (size_t)((tile * 2 + kh) * 4 + w) * 4096;
  const char* xout = xch + (size_t)((tile * 2 + (kh ^ 1)) * 4 + w) * 4096;
  const u32 xo = lane * 16;
  const u32 rwa = bdst + xo, rwe = lds0 + G2_XOFF + w * 16384 + xo;
  u32 rra[3];
#pragma unroll
  for (int j = 0; j < 3; ++j) rra[j] = lds0 + ((w - 1 - j) & 3) * (3 * G2_BSLOT) + j * 8192 + xo;
  u32x4 pv[4];
  f32x4 tr[8][3], va;
#define G2_LDP                                                                                       \
  _Pragma("unroll") for (int c = 0; c < 4; ++c)                                                      \
    asm volatile("global_load_dwordx4 %0, %1, %2 offset:%3 " XLD                                     \
                 : "=v"(pv[c]) : "v"(xo), "s"(xin), "i"(c * 1024) : "memory")
#define G2_RW(ad, m, n)                                                                              \
  asm volatile("ds_write_b128 %0, %1 offset:%2"                                                      \
               ::"v"(ad), "a"(acc[n][m]), "i"((((m) & 3) - 1) * 8192 + (n) * 1024) : "memory")
#define G2_RRD                                                                                       \
  _Pragma("unroll") for (int n = 0; n < 8; ++n) _Pragma("unroll") for (int j = 0; j < 3; ++j)        \
    asm volatile("ds_read_b128 %0, %1 offset:%2" : "=v"(tr[n][j]) : "v"(rra[j]), "i"(n * 1024) : "memory")
#define G2_RWT(n, c)                                                                                 \
  asm volatile("s_waitcnt lgkmcnt(" #c ")"                                                           \
               : "+v"(tr[n][0]), "+v"(tr[n][1]), "+v"(tr[n][2]), "+v"(tr[n + 1][0]), "+v"(tr[n + 1][1]), \
                 "+v"(tr[n + 1][2]), "+v"(tr[n + 2][0]), "+v"(tr[n + 2][1]), "+v"(tr[n + 2][2]),     \
                 "+v"(tr[n + 3][0]), "+v"(tr[n + 3][1]), "+v"(tr[n + 3][2]) : : "memory")
#define G2_SEND(n)                                                                                   \
  do {                                                                                               \
    const f32x4 vb = (acc[n][0] + tr[n][0]) + (tr[n][1] + tr[n][2]);                                 \
    if ((n) & 1) {                                                                                   \
      u32x4 u = {pk2(va[0], va[1]), pk2(va[2], va[3]), pk2(vb[0], vb[1]), pk2(vb[2], vb[3])};        \
      _Pragma("unroll") for (int r = 0; r < 4; ++r)                   \
        u[r] = u[r] < 0xFFFFFFFEu ? u[r] : 0xFFFFFFFEu;                                              \
      asm volatile("global_store_dwordx4 %0, %1, %2 offset:%3 " XST "\n\ts_nop 1"                    \
                   ::"v"(xo), "v"(u), "s"(xout), "i"(((n) >> 1) * 1024) : "memory");                 \
    } else {                                                                                         \
      va = vb;                                                                                       \
      asm volatile("" : "+v"(va));                                                                   \
    }                                                                                                \
  } while (0)
#define G2_G(st, m, FILL)                                                                            \
  _Pragma("unroll") for (int i = 0; i < 4; ++i) {                                                    \
    asm volatile("v_mfma_f32_16x16x32_bf16 %0, %3, %2, %0\n\tv_mfma_f32_16x16x32_bf16 %1, %4, %2, %1" \
                 : "+a"(acc[2 * i][m]), "+a"(acc[2 * i + 1][m])                                      \
                 : "v"(A[st][m]), "v"(G2_B4(st, 2 * i)), "v"(G2_B4(st, 2 * i + 1)));                 \
    SCHED;                                                                                           \
    FILL;                                                                                            \
    SCHED;                                                                                           \
  }
  asm volatile("s_waitcnt vmcnt(16) lgkmcnt(0)"
               : "+v"(xu[0]), "+v"(xu[1]), "+v"(xu[2]), "+v"(xu[3]), "+v"(xu[4]), "+v"(xu[5]), "+v"(xu[6]),
                 "+v"(xu[7]) : : "memory");
  SCHED;
  G2_G(0, 1, {});
  G2_G(0, 2, {});
  WAIT_V(4);
  G2_G(0, 3, { G2_RDB(1, 1, 2 * i); G2_RDB(1, 1, 2 * i + 1); });
  G2_G(0, 0, {});
  WAIT_L(0);
  SCHED;
  G2_G(1, 1, {});
  G2_G(1, 2, G2_RW(rwa, 1, i));
  G2_G(1, 3, G2_RW(rwa, 1, 4 + i));
  G2_G(1, 0, G2_RW(rwa, 2, i));
  G2_G(0, 5, G2_RW(rwa, 2, 4 + i));
  WAIT_V(0);
  G2_G(1, 5, G2_RW(rwa, 3, i));
  G2_G(0, 6, G2_RW(rwa, 3, 4 + i));
  SYNC_L;
  G2_RRD;
  SCHED;
  asm volatile("" : G2_ACC("+a", 0) : : "memory");
  G2_G(1, 6, {});
  G2_RWT(0, 12);
  G2_G(0, 7, { G2_SEND(i); if (i & 1) { G2_RW(rwe, 5, i - 1); G2_RW(rwe, 5, i); } });
  G2_RWT(4, 4);
  G2_G(1, 7, { G2_SEND(4 + i); G2_RW(rwe, 5, 4 + i); });
  BAR;
  G2_G(0, 4, { G2_RW(rwe, 6, 2 * i); G2_RW(rwe, 6, 2 * i + 1); });
  G2_G(1, 4, { G2_RW(rwa, 7, 2 * i); G2_RW(rwa, 7, 2 * i + 1); });
  G2_LDP;
  f32x4 gbv[8];
#pragma unroll
  for (int n = 0; n < 8; ++n) gbv[n] = *(const f32x4*)(modf + d0 + 16 * n + 4 * g);
#pragma unroll
  for (int j = 0; j < 2; ++j) rra[j] = lds0 + G2_XOFF + ((w - 1 - j) & 3) * 16384 + j * 8192 + xo;
  SCHED;
  asm volatile("s_nop 7\n\ts_nop 7\n\ts_waitcnt lgkmcnt(0)\n\ts_barrier" : G2_ACC("+a", 4) : : "memory");
  G2_RRD;
  f32x4 fin[8];
#pragma unroll
  for (int n = 0; n < 8; ++n) {
    if (n == 0) G2_RWT(0, 12);
    if (n == 4) G2_RWT(4, 0);
    fin[n] = (acc[n][4] + tr[n][0]) + (tr[n][1] + tr[n][2]);
    asm volatile("" : "+v"(fin[n]));
  }
  SCHED;
  for (int spin = 0; spin < (1 << 20); ++spin) {
    asm volatile("s_waitcnt vmcnt(0)"
                 : "+v"(pv[0]), "+v"(pv[1]), "+v"(pv[2]), "+v"(pv[3]) : : "memory");
    u32 mx = 0;
#pragma unroll
    for (int n = 0; n < 4; ++n)
#pragma unroll
      for (int r = 0; r < 4; ++r) mx = mx > pv[n][r] ? mx : pv[n][r];
    if (__all(mx != 0xFFFFFFFFu)) break;
    __builtin_amdgcn_s_sleep(G2_SLEEP);
    G2_LDP;
  }
  {
    char* ob = (char*)out + (size_t)(s0 + 64 * kh + 16 * w + (lane & 15)) * (D_ * 2) + (d0 + 16 * (g & 1) + 8 * (g >> 1)) * 2;
#pragma unroll
    for (int np = 0; np < 4; ++np) {
      u32 o[2][2];
#pragma unroll
      for (int mm = 0; mm < 2; ++mm) {
        const int n = 2 * np + mm;
        const f32x4 v = fin[n] + (f32x4){bflo(pv[np][2 * mm]), bfhi(pv[np][2 * mm]), bflo(pv[np][2 * mm + 1]),
                                         bfhi(pv[np][2 * mm + 1])};
        o[mm][0] = pk2(fmaf(gbv[n][0], v[0], bflo(xu[n][0])), fmaf(gbv[n][1], v[1], bfhi(xu[n][0])));
        o[mm][1] = pk2(fmaf(gbv[n][2], v[2], bflo(xu[n][1])), fmaf(gbv[n][3], v[3], bfhi(xu[n][1])));
      }
      auto e0 = __builtin_amdgcn_permlane16_swap(o[0][0], o[1][0], false, false);
      auto e1 = __builtin_amdgcn_permlane16_swap(o[0][1], o[1][1], false, false);
      u32x4 ov = {e0[0], e1[0], e0[1], e1[1]};
      *(u32x4*)(ob + np * 64) = ov;
    }
  }
}

extern "C" void kernel_launch(void* const* d_in, const int* in_sizes, int n_in, void* d_out, int out_size,
                              void* d_ws, size_t ws_size, hipStream_t stream) {
  const u16* x = (const u16*)d_in[0];
  const u16* k = (const u16*)d_in[1];
  const u16* v = (const u16*)d_in[2];
  const u16* shk = (const u16*)d_in[3];
  const u16* ssmk = (const u16*)d_in[4];
  const u16* ssmD = (const u16*)d_in[5];
  const u16* w1 = (const u16*)d_in[7];
  const u16* w3 = (const u16*)d_in[8];
  const u16* w2 = (const u16*)d_in[9];
  const u16* c = (const u16*)d_in[10];
  const u16* wmod = (const u16*)d_in[11];
  char* ws = (char*)d_ws;
  u16* kvT = (u16*)(ws + WS_KVT);
  u16* xT = (u16*)(ws + WS_XT);
  u16* x1T = (u16*)(ws + WS_X1T);
  u16* apT = (u16*)(ws + WS_APT);
  u16* hh = (u16*)(ws + WS_HHT);
  float* modp = (float*)(ws + WS_MODP);
  f32x2* st1 = (f32x2*)(ws + WS_ST1);
  float* modf = (float*)(ws + WS_MOD);
  f32x2* st2 = (f32x2*)(ws + WS_ST2);
  float* c12 = (float*)(ws + WS_C12);

  (void)hipFuncSetAttribute((const void*)k1_conv, hipFuncAttributeMaxDynamicSharedMemorySize, K1_LDS);
  (void)hipFuncSetAttribute((const void*)g1_gemm, hipFuncAttributeMaxDynamicSharedMemorySize, G1_LDS);
  (void)hipFuncSetAttribute((const void*)g2_gemm, hipFuncAttributeMaxDynamicSharedMemorySize, G2_LDS);

  k0_prep<<<K0_TRB + K0_MODB + K0_STB, 256, 0, stream>>>(x, k, v, shk, c, wmod, kvT, xT, st1, modp, st2);
  k1_conv<<<256, 512, K1_LDS, stream>>>(ssmk, ssmD, kvT, xT, st1, modp, w1, w3, x1T, apT, modf, c12);
  g1_gemm<<<256, 512, G1_LDS, stream>>>(apT, w1, w3, x1T, st1, (unsigned long long*)st2, c12, w2, ws + WS_XCH, hh);
  g2_gemm<<<256, 256, G2_LDS, stream>>>(hh, w2, x1T, modf, ws + WS_XCH, (u16*)d_out);
}
```
